```python
import torch

def _softmax(x, axis=-1):
    return torch.softmax(x, dim=axis)
def _gelu(x):
    return torch.nn.functional.gelu(x, approximate="tanh")
def _silu(x):
    return torch.nn.functional.silu(x)
def _sigmoid(x):
    return torch.sigmoid(x)
def _layernorm(x, axis=-1, eps=1e-5):
    m = x.mean(dim=axis, keepdim=True)
    v = x.var(dim=axis, keepdim=True, unbiased=False)
    return (x - m) / torch.sqrt(v + eps)
def _scatter_add(tgt, idx, src, axis=0):
    return tgt.clone().index_add_(axis, idx, src)
def _segment_sum(src, seg, n):
    out = torch.zeros((n,) + src.shape[1:], dtype=src.dtype)
    return out.index_add_(0, seg, src)
def _pad(x, pw):
    flat = tuple(v for pair in reversed(pw) for v in pair)
    return torch.nn.functional.pad(x, flat)

kernel_name = 'standalone__adm_resblock__diffusion'

def setup_inputs():

    _w = (lambda a: (lambda t: t.to(torch.bfloat16) if t.is_floating_point() else t)(torch.as_tensor(a)))
    B, C, HW, E = 2, 512, 32, 768
    sc = (9.0 * C) ** -0.5
    return {
        "x": _w((lambda *s: torch.randn(*s, dtype=torch.bfloat16) * 0.05)(B, C, HW, HW)),
        "t_emb": _w((lambda *s: torch.randn(*s, dtype=torch.bfloat16) * 0.4)(B, E)),
        "g1": _w(1.0 + 0.1 * (lambda *s: torch.randn(*s, dtype=torch.bfloat16) * 0.4)(C)),
        "be1": _w(0.1 * (lambda *s: torch.randn(*s, dtype=torch.bfloat16) * 0.4)(C)),
        "w1": _w((lambda *s: torch.randn(*s, dtype=torch.bfloat16) * 0.4)(C, C, 3, 3) * sc),
        "w_emb": _w((lambda *s: torch.randn(*s, dtype=torch.bfloat16) * 0.4)(E, C) * (E ** -0.5)),
        "g2": _w(1.0 + 0.1 * (lambda *s: torch.randn(*s, dtype=torch.bfloat16) * 0.4)(C)),
        "be2": _w(0.1 * (lambda *s: torch.randn(*s, dtype=torch.bfloat16) * 0.4)(C)),
        "w2": _w((lambda *s: torch.randn(*s, dtype=torch.bfloat16) * 0.4)(C, C, 3, 3) * sc),
    }

def reference(x, t_emb, g1, be1, w1, w_emb, g2, be2, w2):
    B, C, H, W = x.shape
    G, eps = 4, 1e-5
    def gn(z, gamma, beta):
        zr = z.reshape(B, G, (C // G) * H * W)
        mu = zr.mean(-1).reshape(B, G, 1)
        var = ((zr - mu) ** 2).mean(-1).reshape(B, G, 1)
        zn = ((zr - mu) / torch.sqrt(var + eps)).reshape(B, C, H, W)
        return zn * gamma.reshape(1, C, 1, 1) + beta.reshape(1, C, 1, 1)
    def conv3(z, w):
        zp = _pad(z, ((0, 0), (0, 0), (1, 1), (1, 1)))
        taps = [torch.einsum("bchw,oc->bohw", zp[:, :, i:i+H, j:j+W], w[:, :, i, j]) for i in range(3) for j in range(3)]
        out = taps[0]
        for t in taps[1:]:
            out = out + t
        return out
    h = conv3(_silu(gn(x, g1, be1)), w1)
    emb = _silu(t_emb) @ w_emb
    h = h + emb.reshape(B, C, 1, 1)
    h = conv3(_silu(gn(h, g2, be2)), w2)
    return x + h
```

```cpp
#include <hip/hip_runtime.h>
#include <hip/hip_bf16.h>
typedef __hip_bfloat16 bf16;
constexpr int B=2,C=512,H=32,W=32,E=768,G=4,HW=H*W;

__global__ void stats_k(const float* z, float* st){
  int bg=blockIdx.x; const float* p=z+(size_t)bg*(C/G)*HW; int n=(C/G)*HW;
  __shared__ float red[1024];
  float s=0; for(int i=threadIdx.x;i<n;i+=1024) s+=p[i];
  red[threadIdx.x]=s; __syncthreads();
  for(int o=512;o>0;o>>=1){ if(threadIdx.x<o) red[threadIdx.x]+=red[threadIdx.x+o]; __syncthreads(); }
  float mu=red[0]/n; __syncthreads();
  s=0; for(int i=threadIdx.x;i<n;i+=1024){ float d=p[i]-mu; s+=d*d; }
  red[threadIdx.x]=s; __syncthreads();
  for(int o=512;o>0;o>>=1){ if(threadIdx.x<o) red[threadIdx.x]+=red[threadIdx.x+o]; __syncthreads(); }
  if(threadIdx.x==0){ st[bg*2]=mu; st[bg*2+1]=rsqrtf(red[0]/n+1e-5f); }
}
__global__ void cvt_k(const bf16* x, float* z, int n){ int i=blockIdx.x*256+threadIdx.x; if(i<n) z[i]=__bfloat162float(x[i]); }
__global__ void norm_k(const float* z, const float* st, const bf16* g, const bf16* be, float* a, int n){
  int i=blockIdx.x*256+threadIdx.x; if(i>=n) return;
  int c=(i/HW)%C, b=i/(HW*C); int bg=b*G+c/(C/G);
  float y=(z[i]-st[bg*2])*st[bg*2+1]*__bfloat162float(g[c])+__bfloat162float(be[c]);
  y=y/(1.f+__expf(-y));
  a[i]=__bfloat162float(__float2bfloat16(y));
}
__global__ void emb_k(const bf16* t, const bf16* w, float* emb){
  int i=blockIdx.x*256+threadIdx.x; if(i>=B*C) return; int b=i/C,c=i%C; float s=0;
  for(int e=0;e<E;++e){ float v=__bfloat162float(t[b*E+e]); v=v/(1.f+__expf(-v)); v=__bfloat162float(__float2bfloat16(v)); s+=v*__bfloat162float(w[e*C+c]); }
  emb[i]=s;
}
__global__ void conv_k(const float* a, const bf16* w, const float* emb, const bf16* x, float* outf, bf16* outb, int mode){
  int i=blockIdx.x*256+threadIdx.x; int p=i%HW, o=(i/HW)%C, b=i/(HW*C); int y=p/W, xx=p%W;
  float s=0;
  for(int c=0;c<C;++c){
    const float* ap=a+((size_t)b*C+c)*HW; const bf16* wp=w+((size_t)o*C+c)*9;
    for(int dy=0;dy<3;++dy){ int yy=y+dy-1; if(yy<0||yy>=H) continue;
      for(int dx=0;dx<3;++dx){ int x2=xx+dx-1; if(x2<0||x2>=W) continue; s+=ap[yy*W+x2]*__bfloat162float(wp[dy*3+dx]); } }
  }
  if(mode==0) outf[i]=s+emb[b*C+o]; else outb[i]=__float2bfloat16(__bfloat162float(x[i])+s);
}
extern "C" void kernel_launch(void* const* d_in, const int* in_sizes, int n_in, void* d_out, int out_size, void* d_ws, size_t ws_size, hipStream_t stream){
  const bf16* x=(const bf16*)d_in[0]; const bf16* t=(const bf16*)d_in[1]; const bf16* g1=(const bf16*)d_in[2]; const bf16* be1=(const bf16*)d_in[3];
  const bf16* w1=(const bf16*)d_in[4]; const bf16* we=(const bf16*)d_in[5]; const bf16* g2=(const bf16*)d_in[6]; const bf16* be2=(const bf16*)d_in[7]; const bf16* w2=(const bf16*)d_in[8];
  int n=B*C*HW; float* z=(float*)d_ws; float* a=z+n; float* h=a+n; float* st=h+n; float* emb=st+64;
  cvt_k<<<n/256,256,0,stream>>>(x,z,n);
  stats_k<<<8,1024,0,stream>>>(z,st);
  norm_k<<<n/256,256,0,stream>>>(z,st,g1,be1,a,n);
  emb_k<<<4,256,0,stream>>>(t,we,emb);
  conv_k<<<n/256,256,0,stream>>>(a,w1,emb,x,h,nullptr,0);
  stats_k<<<8,1024,0,stream>>>(h,st);
  norm_k<<<n/256,256,0,stream>>>(h,st,g2,be2,a,n);
  conv_k<<<n/256,256,0,stream>>>(a,w2,emb,x,nullptr,(bf16*)d_out,1);
}
```

```cpp
#include <hip/hip_runtime.h>
#include <hip/hip_bf16.h>

typedef unsigned int u32;
typedef unsigned long long u64;
typedef __attribute__((ext_vector_type(2))) __bf16 bf2;
typedef __attribute__((ext_vector_type(8))) __bf16 bf8;
typedef __attribute__((ext_vector_type(4))) u32 u32x4;
typedef __attribute__((ext_vector_type(4))) float f32x4;
typedef __attribute__((ext_vector_type(4))) short s16x4;
#define LDSP(T, a) ((__attribute__((address_space(3))) T*)(size_t)(a))

constexpr u32 WS_IMG = 512u << 10;
constexpr u32 WS_GEN = 0;
constexpr u32 WS_GRAN = 16384;
constexpr u32 WS_FLAG = 5 * 16384;
constexpr u32 WS_EMB = 8 * 16384;
constexpr u32 WS_ACT = 4u << 20;
constexpr u32 ACT_STRIDE = 8u << 20;
constexpr u32 WS_END = 20u << 20;
constexpr u32 WREG = 40960, RING = 18432, SVEC = RING + 12288;


__device__ __forceinline__ u32 f2u(float f) { return __builtin_bit_cast(u32, f); }
__device__ __forceinline__ float u2f(u32 u) { return __builtin_bit_cast(float, u); }
__device__ __forceinline__ u32 pack_bf(float a, float b) {
  bf2 p; p[0] = (__bf16)a; p[1] = (__bf16)b; return __builtin_bit_cast(u32, p);
}
__device__ __forceinline__ float silu(float y) {
  return y * __builtin_amdgcn_rcpf(1.f + __builtin_amdgcn_exp2f(-1.4426950408889634f * y));
}
__device__ __forceinline__ u32x4 make_srd(const void* p, u32 bytes) {
  u64 a = (u64)p;
  u32x4 r;
  r[0] = (u32)a; r[1] = (u32)(a >> 32); r[2] = bytes; r[3] = 0x00020000u;
  return r;
}
#define GET_TAG(t, n)                                                                                              \
  asm volatile("s_waitcnt vmcnt(" #n ")\n\tv_readfirstlane_b32 %0, v127\n\ts_nop 3\n\ts_mul_i32 %0, %0, 0x9E3779B1\n\t" \
               "s_add_u32 %0, %0, 0x7F4A7C15" : "=s"(t) :: "memory", "scc")

__global__ __launch_bounds__(256, 1) __attribute__((amdgpu_num_vgpr(48)))
void k_resblock(const char* __restrict__ x, const char* __restrict__ t_emb, const char* __restrict__ g1,
                const char* __restrict__ be1, const char* __restrict__ w1, const char* __restrict__ w_emb,
                const char* __restrict__ g2, const char* __restrict__ be2, const char* __restrict__ w2,
                char* __restrict__ out, char* __restrict__ ws) {
  extern __shared__ __attribute__((aligned(16))) char smem[];
  u64 pc;
  asm volatile("s_getpc_b64 %0\n.Lpf_here:" : "=s"(pc));
  const int bid = blockIdx.x, r = bid >> 3, b = (bid >> 2) & 1, g = bid & 3;
  const int cc = r & 3, nt = g * 4 + cc, mt = r >> 2, o0 = nt * 32;
  const u32 order = (((0xE79u >> ((mt % 3) * 2)) & 0x3fu) << 2) | (((0xE4E4u >> ((mt & 3) * 2)) & 0xffu) << 8);
  const int lane = threadIdx.x & 63, wid = __builtin_amdgcn_readfirstlane(threadIdx.x >> 6);
  const u32 lds0 = (u32)(size_t)(__attribute__((address_space(3))) char*)smem;
  const u32 wb = lds0 + wid * WREG;
  if (wid == 2 && mt == 1)
    asm volatile(
      "v_lshlrev_b32 v254, 7, %[lane]\n\t"
      "v_add_u32 v253, 0x2000, v254\n\t"
      "v_min_u32 v254, .Lpf_end-.Lpf_here, v254\n\t"
      "v_min_u32 v253, .Lpf_end-.Lpf_here, v253\n\t"
      "global_load_dword v252, v254, %[pc]\n\t"
      "global_load_dword v252, v253, %[pc]\n\t"
        ::[lane] "v"(lane), [pc] "s"(pc) : "memory");
  asm volatile(
      "v_and_b32 v113, 15, %[lane]\n\t"
      "v_lshrrev_b32 v114, 4, %[lane]\n\t"
      "v_lshlrev_b32 v112, 4, %[lane]\n\t"
      "v_lshl_add_u32 v65, v113, 4, %[xo]\n\t"
      "v_lshl_add_u32 v184, v114, 11, v65\n\t"
      "v_add_u32 v185, 8192, v184\n\t"
      "v_add_u32 v186, 16384, v184\n\t"
      "v_add_u32 v187, 24576, v184\n\t"
      "v_add_u32 v188, 32768, v184\n\t"
      "v_add_u32 v189, 40960, v184\n\t"
      "v_add_u32 v190, 49152, v184\n\t"
      "v_add_u32 v191, 57344, v184\n\t"
      "v_mov_b32 v80, %[lane]\n\t"
      "v_lshrrev_b32 v66, 2, %[lane]\n\t"
      "v_and_b32 v67, 48, v112\n\t"
      "s_lshl_b32 s64, %[wid], 6\n\t"
      "s_add_u32 s64, s64, %[xo]\n\t"
      "v_lshl_or_b32 v68, v66, 11, v67\n\t"
      "v_add_u32 v68, s64, v68\n\t"
      "v_add_u32 v69, 0x8000, v68\n\t"
      "v_lshl_add_u32 v70, v113, 2, %[po]\n\t"
      ::[lane] "v"(lane), [xo] "s"(((b * 512 + o0) * 1024 + mt * 128) * 2), [wid] "s"(wid), [po] "s"(o0 * 2)
      : "scc", "s64", "v255");
  asm volatile("" ::"s"(x), "s"(t_emb), "s"(g1), "s"(be1), "s"(w1), "s"(w_emb), "s"(g2), "s"(be2), "s"(w2), "s"(out), "s"(ws));
  const u32x4 wss = make_srd(ws + b * WS_IMG, WS_END - WS_IMG);
  const bool pblock = g == 0 && r < 4;
  u64 pt0, pt1;
  if (pblock) {
    if (wid == 3)
      asm volatile(".p2align 6\n\ts_memtime %0\n\tbuffer_store_dword v0, off, %3, %2 sc1\n\ts_barrier\n\t"
                   "s_waitcnt vmcnt(0)\n\ts_memtime %1\n\ts_waitcnt lgkmcnt(0)"
                   : "=&s"(pt0), "=&s"(pt1) : "s"(WS_GEN + 1024 + (r & 1) * 8192 + r * 128), "s"(wss) : "memory");
    else
      __builtin_amdgcn_s_barrier();
  }
  asm volatile(
      "s_cmp_lg_u32 %[wid], 0\n\t"
      "s_cbranch_scc1 1f\n\t"
      "global_load_dwordx4 v[220:223], v184, %[xb]\n\t"
      "global_load_dwordx4 v[224:227], v185, %[xb]\n\t"
      "global_load_dwordx4 v[228:231], v186, %[xb]\n\t"
      "global_load_dwordx4 v[232:235], v187, %[xb]\n\t"
      "global_load_dwordx4 v[236:239], v188, %[xb]\n\t"
      "global_load_dwordx4 v[240:243], v189, %[xb]\n\t"
      "global_load_dwordx4 v[244:247], v190, %[xb]\n\t"
      "global_load_dwordx4 v[248:251], v191, %[xb]\n\t"
      "1:\n\t"
      "s_add_u32 m0, %[wb], 28672\n\t"
      "buffer_load_dword v127, off, %[wss], 0 sc1\n\t"
      "global_load_lds_dwordx4 v68, %[xb]\n\t"
      "s_add_u32 m0, %[wb], 29696\n\t"
      "s_nop 0\n\t"
      "global_load_lds_dwordx4 v69, %[xb]\n\t"
      "s_add_u32 m0, %[wb], 40384\n\t"
      "s_mov_b64 exec, 0xffff\n\t"
      "global_load_lds_dword v70, %[g1]\n\t"
      "s_lshl_b64 exec, exec, 16\n\t"
      "global_load_lds_dword v70, %[be1]\n\t"
      "s_lshl_b64 exec, exec, 16\n\t"
      "global_load_lds_dword v70, %[g2]\n\t"
      "s_lshl_b64 exec, exec, 16\n\t"
      "global_load_lds_dword v70, %[be2]\n\t"
      "s_mov_b64 exec, -1\n\t"
      ::[xb] "s"(x), [wss] "s"(wss), [wid] "s"(wid), [wb] "s"(wb),
        [g1] "s"(g1), [be1] "s"(be1), [g2] "s"(g2), [be2] "s"(be2)
      : "memory", "scc", "vcc", "s64", "s65", "v255");
  if (pblock && wid == 3) {
    u32 tg;
    GET_TAG(tg, 6);
    const u32x4 ch = {(u32)(pt1 - pt0), tg, (u32)(pt1 - pt0), tg};
    if (lane < 8)
      asm volatile("buffer_store_dwordx4 %0, %1, %2, %3 offen sc1\n\ts_nop 1"
                   ::"v"(ch), "v"((lane >> 1) * 128 + (lane & 1) * 16), "s"(wss), "s"(WS_GEN + 2048 + r * 32) : "memory");
  }
  if (wid == 1)
    asm volatile(
      "v_min_u32 v68, 47, v80\n\t"
      "s_add_u32 m0, %[wb], 30720\n\t"
      "v_lshlrev_b32 v68, 2, v68\n\t"
      "v_lshl_or_b32 v69, v66, 10, v67\n\t"
      "global_load_lds_dword v68, %[te]\n\t"
      "s_mov_b64 s[64:65], %[we]\n\t"
      "s_add_u32 m0, %[wb], 18432\n\t"
      "s_nop 0\n\t"
      "global_load_lds_dwordx4 v69, s[64:65]\n\t"
      "s_add_u32 s64, s64, 0x4000\n\t"
      "s_addc_u32 s65, s65, 0\n\t"
      "s_add_u32 m0, %[wb], 19456\n\t"
      "s_nop 0\n\t"
      "global_load_lds_dwordx4 v69, s[64:65]\n\t"
      "s_add_u32 s64, s64, 0x4000\n\t"
      "s_addc_u32 s65, s65, 0\n\t"
      "s_add_u32 m0, %[wb], 20480\n\t"
      "s_nop 0\n\t"
      "global_load_lds_dwordx4 v69, s[64:65]\n\t"
      "s_add_u32 s64, s64, 0x4000\n\t"
      "s_addc_u32 s65, s65, 0\n\t"
      "s_add_u32 m0, %[wb], 21504\n\t"
      "s_nop 0\n\t"
      "global_load_lds_dwordx4 v69, s[64:65]\n\t"
      "s_add_u32 s64, s64, 0x4000\n\t"
      "s_addc_u32 s65, s65, 0\n\t"
      "s_add_u32 m0, %[wb], 22528\n\t"
      "s_nop 0\n\t"
      "global_load_lds_dwordx4 v69, s[64:65]\n\t"
      "s_add_u32 s64, s64, 0x4000\n\t"
      "s_addc_u32 s65, s65, 0\n\t"
      "s_add_u32 m0, %[wb], 23552\n\t"
      "s_nop 0\n\t"
      "global_load_lds_dwordx4 v69, s[64:65]\n\t"
        ::[te] "s"(t_emb + (b * 768 + mt * 96) * 2), [we] "s"(w_emb + ((size_t)mt * 96 * 512 + o0) * 2), [wb] "s"(wb)
        : "memory", "scc", "s64", "s65");
  const u32 gbase = WS_GRAN + (g >> 1) * 16384 + (g & 1) * 4096;
  asm volatile(
      "v_mul_u32_u24 v68, 1821, v80\n\t"
      "s_movk_i32 s64, 8640\n\t"
      "v_add_u32 v70, 0x1000, v112\n\t"
      "v_add_u32 v71, 0x2000, v112\n\t"
      "v_add_u32 v129, 116544, v68\n\t"
      "v_add_u32 v130, 233088, v68\n\t"
      "v_add_u32 v131, 349632, v68\n\t"
      "v_add_u32 v132, 466176, v68\n\t"
      "v_add_u32 v133, 582720, v68\n\t"
      "v_add_u32 v134, 699264, v68\n\t"
      "v_add_u32 v135, 815808, v68\n\t"
      "v_add_u32 v136, 932352, v68\n\t"
      "v_lshrrev_b32 v128, 16, v68\n\t"
      "v_lshrrev_b32 v129, 16, v129\n\t"
      "v_lshrrev_b32 v130, 16, v130\n\t"
      "v_lshrrev_b32 v131, 16, v131\n\t"
      "v_lshrrev_b32 v132, 16, v132\n\t"
      "v_lshrrev_b32 v133, 16, v133\n\t"
      "v_lshrrev_b32 v134, 16, v134\n\t"
      "v_lshrrev_b32 v135, 16, v135\n\t"
      "v_lshrrev_b32 v136, 16, v136\n\t"
      "v_and_b32 v184, 4, v128\n\t"
      "v_and_b32 v185, 4, v129\n\t"
      "v_and_b32 v186, 4, v130\n\t"
      "v_and_b32 v187, 4, v131\n\t"
      "v_and_b32 v188, 4, v132\n\t"
      "v_and_b32 v189, 4, v133\n\t"
      "v_and_b32 v190, 4, v134\n\t"
      "v_and_b32 v191, 4, v135\n\t"
      "v_and_b32 v192, 4, v136\n\t"
      "v_mad_u32_u24 v128, v128, s64, v112\n\t"
      "v_mad_u32_u24 v129, v129, s64, v112\n\t"
      "v_mad_u32_u24 v130, v130, s64, v112\n\t"
      "v_mad_u32_u24 v131, v131, s64, v112\n\t"
      "v_mad_u32_u24 v132, v132, s64, v70\n\t"
      "v_mad_u32_u24 v133, v133, s64, v70\n\t"
      "v_mad_u32_u24 v134, v134, s64, v70\n\t"
      "v_mad_u32_u24 v135, v135, s64, v70\n\t"
      "v_mad_u32_u24 v136, v136, s64, v71\n\t"
      "v_lshlrev_b32 v184, 3, v184\n\t"
      "v_lshlrev_b32 v185, 3, v185\n\t"
      "v_lshlrev_b32 v186, 3, v186\n\t"
      "v_lshlrev_b32 v187, 3, v187\n\t"
      "v_lshlrev_b32 v188, 3, v188\n\t"
      "v_lshlrev_b32 v189, 3, v189\n\t"
      "v_lshlrev_b32 v190, 3, v190\n\t"
      "v_lshlrev_b32 v191, 3, v191\n\t"
      "v_lshlrev_b32 v192, 3, v192\n\t"
      "v_xor_b32 v128, v128, v184\n\t"
      "v_xor_b32 v129, v129, v185\n\t"
      "v_xor_b32 v130, v130, v186\n\t"
      "v_xor_b32 v131, v131, v187\n\t"
      "v_xor_b32 v132, v132, v188\n\t"
      "v_xor_b32 v133, v133, v189\n\t"
      "v_xor_b32 v134, v134, v190\n\t"
      "v_xor_b32 v135, v135, v191\n\t"
      "v_xor_b32 v136, v136, v192\n\t"
      "v_lshl_or_b32 v115, v66, 7, v67\n\t"
      "v_lshlrev_b32 v116, 1, v67\n\t"
      "v_add_u32 v115, %[gbase], v115\n\t"
      "v_add_u32 v116, %[gn], v116\n\t"
      "v_mov_b32 v118, %[lds0]\n\t"
      ::[gbase] "s"(gbase), [gn] "s"(WS_GEN + 2048 + g * 128), [lds0] "s"(lds0) : "s64");
  const u32x4 w1srd = make_srd(w1 + (size_t)o0 * 9216, 32 * 9216);
  u32 tag;
  if (wid == 0) {
    asm volatile(
      "v_mov_b32 v64, 0\n\t"
      "v_mov_b32 v65, 0\n\t"
      "v_mov_b32 v66, 0\n\t"
      "v_mov_b32 v67, 0\n\t"
      "v_mov_b32 v68, 0\n\t"
      "v_mov_b32 v69, 0\n\t"
      "v_mov_b32 v70, 0\n\t"
      "v_mov_b32 v71, 0\n\t"
      "v_and_b32 v81, 1, v113\n\t"
      "v_and_b32 v85, 2, v113\n\t"
      "v_lshlrev_b32 v81, 13, v81\n\t"
      "s_mov_b32 s64, 0x3f803f80\n\t"
      "v_lshl_or_b32 v81, v114, 4, v81\n\t"
      "v_lshl_or_b32 v81, v85, 5, v81\n\t"
      "s_waitcnt vmcnt(6)\n\t"
      "v_readfirstlane_b32 %[tag], v127\n\t"
      "v_dot2c_f32_bf16 v64, s64, v220\n\t"
      "v_dot2c_f32_bf16 v68, v220, v220\n\t"
      "v_dot2c_f32_bf16 v65, s64, v221\n\t"
      "v_dot2c_f32_bf16 v69, v221, v221\n\t"
      "v_dot2c_f32_bf16 v66, s64, v222\n\t"
      "v_dot2c_f32_bf16 v70, v222, v222\n\t"
      "s_mul_i32 %[tag], %[tag], 0x9E3779B1\n\t"
      "v_dot2c_f32_bf16 v67, s64, v223\n\t"
      "v_dot2c_f32_bf16 v71, v223, v223\n\t"
      "v_dot2c_f32_bf16 v64, s64, v224\n\t"
      "v_dot2c_f32_bf16 v68, v224, v224\n\t"
      "s_add_u32 %[tag], %[tag], 0x7F4A7C15\n\t"
      "v_dot2c_f32_bf16 v65, s64, v225\n\t"
      "v_dot2c_f32_bf16 v69, v225, v225\n\t"
      "v_dot2c_f32_bf16 v66, s64, v226\n\t"
      "v_dot2c_f32_bf16 v70, v226, v226\n\t"
      "v_dot2c_f32_bf16 v67, s64, v227\n\t"
      "v_dot2c_f32_bf16 v71, v227, v227\n\t"
      "v_dot2c_f32_bf16 v64, s64, v228\n\t"
      "v_dot2c_f32_bf16 v68, v228, v228\n\t"
      "v_dot2c_f32_bf16 v65, s64, v229\n\t"
      "v_dot2c_f32_bf16 v69, v229, v229\n\t"
      "v_dot2c_f32_bf16 v66, s64, v230\n\t"
      "v_dot2c_f32_bf16 v70, v230, v230\n\t"
      "v_dot2c_f32_bf16 v67, s64, v231\n\t"
      "v_dot2c_f32_bf16 v71, v231, v231\n\t"
      "v_dot2c_f32_bf16 v64, s64, v232\n\t"
      "v_dot2c_f32_bf16 v68, v232, v232\n\t"
      "v_dot2c_f32_bf16 v65, s64, v233\n\t"
      "v_dot2c_f32_bf16 v69, v233, v233\n\t"
      "v_dot2c_f32_bf16 v66, s64, v234\n\t"
      "v_dot2c_f32_bf16 v70, v234, v234\n\t"
      "v_dot2c_f32_bf16 v67, s64, v235\n\t"
      "v_dot2c_f32_bf16 v71, v235, v235\n\t"
      "v_dot2c_f32_bf16 v64, s64, v236\n\t"
      "v_dot2c_f32_bf16 v68, v236, v236\n\t"
      "v_dot2c_f32_bf16 v65, s64, v237\n\t"
      "v_dot2c_f32_bf16 v69, v237, v237\n\t"
      "v_dot2c_f32_bf16 v66, s64, v238\n\t"
      "v_dot2c_f32_bf16 v70, v238, v238\n\t"
      "v_dot2c_f32_bf16 v67, s64, v239\n\t"
      "v_dot2c_f32_bf16 v71, v239, v239\n\t"
      "v_dot2c_f32_bf16 v64, s64, v240\n\t"
      "v_dot2c_f32_bf16 v68, v240, v240\n\t"
      "v_dot2c_f32_bf16 v65, s64, v241\n\t"
      "v_dot2c_f32_bf16 v69, v241, v241\n\t"
      "v_dot2c_f32_bf16 v66, s64, v242\n\t"
      "v_dot2c_f32_bf16 v70, v242, v242\n\t"
      "v_dot2c_f32_bf16 v67, s64, v243\n\t"
      "v_dot2c_f32_bf16 v71, v243, v243\n\t"
      "v_dot2c_f32_bf16 v64, s64, v244\n\t"
      "v_dot2c_f32_bf16 v68, v244, v244\n\t"
      "v_dot2c_f32_bf16 v65, s64, v245\n\t"
      "v_dot2c_f32_bf16 v69, v245, v245\n\t"
      "v_dot2c_f32_bf16 v66, s64, v246\n\t"
      "v_dot2c_f32_bf16 v70, v246, v246\n\t"
      "v_dot2c_f32_bf16 v67, s64, v247\n\t"
      "v_dot2c_f32_bf16 v71, v247, v247\n\t"
      "v_dot2c_f32_bf16 v64, s64, v248\n\t"
      "v_dot2c_f32_bf16 v68, v248, v248\n\t"
      "v_dot2c_f32_bf16 v65, s64, v249\n\t"
      "v_dot2c_f32_bf16 v69, v249, v249\n\t"
      "v_dot2c_f32_bf16 v66, s64, v250\n\t"
      "v_dot2c_f32_bf16 v70, v250, v250\n\t"
      "v_dot2c_f32_bf16 v67, s64, v251\n\t"
      "v_dot2c_f32_bf16 v71, v251, v251\n\t"
      "s_nop 1\n\t"
      "v_pk_add_f32 v[64:65], v[64:65], v[66:67]\n\t"
      "v_pk_add_f32 v[68:69], v[68:69], v[70:71]\n\t"
      "s_nop 0\n\t"
      "v_add_f32 v70, v64, v65\n\t"
      "v_add_f32 v68, v68, v69\n\t"
      "v_mov_b32 v65, %[tag]\n\t"
      "v_mov_b32 v67, %[tag]\n\t"
      "s_nop 0\n\t"
      "v_add_f32_dpp v70, v70, v70 quad_perm:[1,0,3,2] row_mask:0xf bank_mask:0xf\n\t"
      "v_add_f32_dpp v68, v68, v68 quad_perm:[1,0,3,2] row_mask:0xf bank_mask:0xf\n\t"
      "s_nop 0\n\t"
      "v_add_f32_dpp v70, v70, v70 quad_perm:[2,3,0,1] row_mask:0xf bank_mask:0xf\n\t"
      "v_add_f32_dpp v68, v68, v68 quad_perm:[2,3,0,1] row_mask:0xf bank_mask:0xf\n\t"
      "s_nop 0\n\t"
      "v_add_f32_dpp v70, v70, v70 row_half_mirror row_mask:0xf bank_mask:0xf\n\t"
      "v_add_f32_dpp v68, v68, v68 row_half_mirror row_mask:0xf bank_mask:0xf\n\t"
      "s_nop 0\n\t"
      "v_add_f32_dpp v70, v70, v70 row_mirror row_mask:0xf bank_mask:0xf\n\t"
      "v_add_f32_dpp v68, v68, v68 row_mirror row_mask:0xf bank_mask:0xf\n\t"
      "s_mov_b32 exec_lo, 0xf000f\n\t"
      "s_mov_b32 exec_hi, 0xf000f\n\t"
      "v_mul_f32 v64, 0x3a800000, v70\n\t"
      "v_fma_f32 v66, -v70, v64, v68\n\t"
      "v_max_f32 v66, 0, v66\n\t"
      "buffer_store_dwordx4 v[64:67], v81, %[wss], %[so] offen sc1\n\t"
      "s_mov_b64 exec, -1\n\t"
      "s_waitcnt vmcnt(1)\n\t"
        : [tag] "=&s"(tag)
        : [wss] "s"(wss), [so] "s"(gbase + r * 128) : "memory", "scc", "s64");
  } else {
    const int jj = (3 - wid + mt) & 7, tw = jj >> 1, oh = jj & 1;
    asm volatile(
      "s_add_u32 m0, %[dst], 0\n\t"
      "s_nop 0\n\t"
      "buffer_load_dwordx4 v128, %[srd], %[so] offen offset:0 lds\n\t"
      "buffer_load_dwordx4 v129, %[srd], %[so] offen offset:1024 lds\n\t"
      "buffer_load_dwordx4 v130, %[srd], %[so] offen offset:2048 lds\n\t"
      "buffer_load_dwordx4 v131, %[srd], %[so] offen offset:3072 lds\n\t"
      "s_add_u32 m0, %[dst], 4096\n\t"
      "s_nop 0\n\t"
      "buffer_load_dwordx4 v132, %[srd], %[so] offen offset:0 lds\n\t"
      "buffer_load_dwordx4 v133, %[srd], %[so] offen offset:1024 lds\n\t"
      "buffer_load_dwordx4 v134, %[srd], %[so] offen offset:2048 lds\n\t"
      "buffer_load_dwordx4 v135, %[srd], %[so] offen offset:3072 lds\n\t"
      "s_add_u32 m0, %[dst], 8192\n\t"
      "s_nop 0\n\t"
      "buffer_load_dwordx4 v136, %[srd], %[so] offen offset:0 lds\n\t"
        ::[dst] "s"(lds0 + tw * WREG + oh * 9216), [srd] "s"(w1srd), [so] "s"(tw * 2304 + oh * 147456) : "memory", "scc");
  }
  asm volatile(
      "v_lshrrev_b32 v64, 2, v113\n\t"
      "v_and_b32 v65, 3, v80\n\t"
      "v_lshl_or_b32 v64, v114, 2, v64\n\t"
      "v_lshlrev_b32 v66, 3, v65\n\t"
      "v_lshl_or_b32 v64, v64, 6, v66\n\t"
      "v_add_u32 v82, %[wb], v64\n\t"
      "s_cmp_eq_u32 %[mt], 0\n\t"
      "s_cselect_b32 s64, 2.0, 6144\n\t"
      "s_cmp_eq_u32 %[mt], 7\n\t"
      "s_cselect_b32 s65, 2.0, 0x8000\n\t"
      "v_add_u32 v137, s64, v112\n\t"
      "v_add_u32 v142, s65, v112\n\t"
      "v_add_u32 v138, 16384, v112\n\t"
      "v_add_u32 v139, 18432, v112\n\t"
      "v_add_u32 v140, 20480, v112\n\t"
      "v_add_u32 v141, 22528, v112\n\t"
      "s_add_u32 s64, %[wb], 18496\n\t"
      "v_add_u32 v67, -1, v113\n\t"
      "v_lshrrev_b32 v68, 1, v67\n\t"
      "v_and_b32 v68, 2, v68\n\t"
      "v_xor_b32 v69, v68, v114\n\t"
      "v_lshlrev_b32 v69, 4, v69\n\t"
      "v_lshl_add_u32 v69, v67, 6, v69\n\t"
      "v_add_u32 v143, s64, v69\n\t"
      "v_add_u32 v67, 0, v113\n\t"
      "v_lshrrev_b32 v68, 1, v67\n\t"
      "v_and_b32 v68, 2, v68\n\t"
      "v_xor_b32 v69, v68, v114\n\t"
      "v_lshlrev_b32 v69, 4, v69\n\t"
      "v_lshl_add_u32 v69, v67, 6, v69\n\t"
      "v_add_u32 v144, s64, v69\n\t"
      "v_lshrrev_b32 v70, 1, v114\n\t"
      "v_xor_b32 v71, v70, v68\n\t"
      "v_lshlrev_b32 v71, 4, v71\n\t"
      "v_lshl_add_u32 v71, v113, 6, v71\n\t"
      "v_and_b32 v121, 1, v114\n\t"
      "v_lshl_add_u32 v71, v121, 3, v71\n\t"
      "s_add_u32 s65, %[wb], 28672\n\t"
      "v_add_u32 v120, s65, v71\n\t"
      "v_add_u32 v67, 1, v113\n\t"
      "v_lshrrev_b32 v68, 1, v67\n\t"
      "v_and_b32 v68, 2, v68\n\t"
      "v_xor_b32 v69, v68, v114\n\t"
      "v_lshlrev_b32 v69, 4, v69\n\t"
      "v_lshl_add_u32 v69, v67, 6, v69\n\t"
      "v_add_u32 v145, s64, v69\n\t"
      "v_mul_u32_u24 v67, 1088, v121\n\t"
      "v_mul_u32_u24 v68, 40960, v70\n\t"
      "v_lshl_add_u32 v67, v113, 1, v67\n\t"
      "s_lshl_b32 s64, %[wid], 6\n\t"
      "s_add_u32 s64, s64, 34816\n\t"
      "s_add_u32 s64, s64, %[lds0]\n\t"
      "v_add3_u32 v72, v67, v68, s64\n\t"
      "v_add_u32 v73, 81920, v72\n\t"
      "v_xor_b32 v121, 32, v120\n\t"
      "v_mul_u32_u24 v67, 576, v113\n\t"
      "v_mul_u32_u24 v68, 144, v114\n\t"
      "v_add_u32 v67, %[wb], v67\n\t"
      "v_and_b32 v69, 4, v113\n\t"
      "v_lshlrev_b32 v69, 3, v69\n\t"
      "v_xor_b32 v71, v68, v69\n\t"
      "v_add_u32 v89, v67, v71\n\t"
      "v_add_u32 v71, 16, v68\n\t"
      "v_xor_b32 v71, v71, v69\n\t"
      "v_add3_u32 v90, v67, v71, -16\n\t"
      "v_add_u32 v71, 32, v68\n\t"
      "v_xor_b32 v71, v71, v69\n\t"
      "v_add_u32 v91, v67, v71\n\t"
      "v_subrev_u32 v91, 32, v91\n\t"
      "v_add_u32 v71, 48, v68\n\t"
      "v_xor_b32 v71, v71, v69\n\t"
      "v_add_u32 v92, v67, v71\n\t"
      "v_subrev_u32 v92, 48, v92\n\t"
      "s_add_u32 s64, %[wb], 18432\n\t"
      "v_add_u32 v147, s64, v112\n\t"
      "v_lshrrev_b32 v67, 2, v80\n\t"
      "v_min_u32 v67, 6, v67\n\t"
      "v_mul_u32_u24 v67, 2112, v67\n\t"
      "v_lshl_add_u32 v67, v65, 4, v67\n\t"
      "v_add_u32 v123, s64, v67\n\t"
      "s_add_u32 s64, %[wb], 40384\n\t"
      "v_lshl_add_u32 v117, v114, 3, s64\n\t"
      "v_mul_u32_u24 v67, 40960, v65\n\t"
      "v_add_u32 v119, %[lds0], v67\n\t"
      "v_and_b32 v67, 7, v113\n\t"
      "v_lshlrev_b32 v67, 8, v67\n\t"
      "v_lshl_or_b32 v122, v114, 5, v67\n\t"
      "v_min_u32 v67, 2, v114\n\t"
      "s_sub_u32 s64, %[mt], 1\n\t"
      "v_add_u32 v67, s64, v67\n\t"
      "v_med3_i32 v67, v67, 0, 7\n\t"
      "v_lshl_or_b32 v67, v67, 4, v113\n\t"
      "v_lshlrev_b32 v124, 6, v67\n\t"
      "s_lshl_b32 s64, %[wid], 11\n\t"
      "s_add_u32 s64, s64, 18432\n\t"
      "s_add_u32 s64, s64, %[lds0]\n\t"
      "v_add_u32 v125, s64, v112\n\t"
      "v_add_u32 v126, 81920, v125\n\t"
      "v_lshlrev_b32 v67, 4, v113\n\t"
      "v_mul_u32_u24 v68, 272, v114\n\t"
      "s_add_u32 s64, %[wb], 34816\n\t"
      "v_add3_u32 v74, v67, v68, s64\n\t"
      "s_lshl_b32 s64, %[wid], 14\n\t"
      "v_lshl_add_u32 v67, v114, 11, v67\n\t"
      "v_add_u32 v75, s64, v67\n\t"
      "v_mov_b32 v76, 0\n\t"
      "v_mov_b32 v77, 0\n\t"
      "v_mov_b32 v78, 0\n\t"
      "v_mov_b32 v79, 0\n\t"
      ::[wb] "s"(wb), [lds0] "s"(lds0), [wid] "s"(wid), [mt] "s"(mt) : "memory", "scc", "vcc", "s64", "s65");
  if (wid != 0) {
    GET_TAG(tag, 9);
    if (wid == 1) {
      const int l15 = lane & 15, q = lane >> 4;
      const u32 sva = wb + SVEC + lane * 4;
      const u32 tv = *LDSP(u32, sva);
      *LDSP(u32, sva) = pack_bf(silu(u2f(tv << 16)), silu(u2f(tv & 0xffff0000u)));
      f32x4 ea = {0.f, 0.f, 0.f, 0.f}, eb = ea;
      const u32 ta = wb + RING + (8 * q + (l15 >> 2)) * 64 + (l15 & 3) * 8;
#pragma unroll
      for (int ks = 0; ks < 3; ++ks) {
        const bf8 sf = __builtin_bit_cast(bf8, *LDSP(u32x4, wb + SVEC + ks * 64 + q * 16));
        s16x4 t0 = __builtin_amdgcn_ds_read_tr16_b64_v4i16(LDSP(s16x4, ta + ks * 2048));
        s16x4 t1 = __builtin_amdgcn_ds_read_tr16_b64_v4i16(LDSP(s16x4, ta + ks * 2048 + 256));
        s16x4 t2 = __builtin_amdgcn_ds_read_tr16_b64_v4i16(LDSP(s16x4, ta + ks * 2048 + 32));
        s16x4 t3 = __builtin_amdgcn_ds_read_tr16_b64_v4i16(LDSP(s16x4, ta + ks * 2048 + 288));
        ea = __builtin_amdgcn_mfma_f32_16x16x32_bf16(__builtin_bit_cast(bf8, __builtin_shufflevector(t0, t1, 0, 1, 2, 3, 4, 5, 6, 7)), sf, ea, 0, 0, 0);
        eb = __builtin_amdgcn_mfma_f32_16x16x32_bf16(__builtin_bit_cast(bf8, __builtin_shufflevector(t2, t3, 0, 1, 2, 3, 4, 5, 6, 7)), sf, eb, 0, 0, 0);
      }
      const f32x4 es = (l15 & 2) ? eb : ea;
      const u32x4 ge = {f2u((l15 & 1) ? es[2] : es[0]), tag, f2u((l15 & 1) ? es[3] : es[1]), tag};
      if (l15 < 8)
        asm volatile("buffer_store_dwordx4 %0, %1, %2, %3 offen sc1\n\ts_nop 1"
                     ::"v"(ge), "v"(q * 32 + (l15 & 2) * 64 + (l15 & 1) * 16 + (l15 >> 2) * 8192), "s"(wss), "s"(WS_EMB + g * 16384 + cc * 2048 + mt * 256) : "memory");
    }
  }
  if (wid == 3 && lane < 32) {
    const u32 nt4 = ~tag;
    const u32x4 f = {nt4, nt4, nt4, nt4};
    const u32 o = (lane & 8 ? WS_FLAG + (lane & 4) * 4096 + (mt * 16 + nt) * 64 + (lane & 3) * 16 : gbase + 32768 + r * 128 + (lane & 7) * 16) + (lane >> 4) * 8192;
    asm volatile("buffer_store_dwordx4 %0, %1, %2, 0 offen sc1\n\ts_nop 1" ::"v"(f), "v"(o), "s"(wss) : "memory");
  }
  asm volatile(
      "ds_read_b64_tr_b16 v[184:185], v82 offset:28672\n\t"
      "ds_read_b64_tr_b16 v[186:187], v82 offset:29696\n\t"
      "ds_read_b64_tr_b16 v[188:189], v82 offset:28704\n\t"
      "ds_read_b64_tr_b16 v[190:191], v82 offset:29728\n\t"
      "s_waitcnt lgkmcnt(0)\n\t"
      "v_lshlrev_b32 v96, 16, v184\n\t"
      "v_and_b32 v97, 0xffff0000, v184\n\t"
      "v_lshlrev_b32 v98, 16, v185\n\t"
      "v_and_b32 v99, 0xffff0000, v185\n\t"
      "v_lshlrev_b32 v100, 16, v186\n\t"
      "v_and_b32 v101, 0xffff0000, v186\n\t"
      "v_lshlrev_b32 v102, 16, v187\n\t"
      "v_and_b32 v103, 0xffff0000, v187\n\t"
      "v_lshlrev_b32 v104, 16, v188\n\t"
      "v_and_b32 v105, 0xffff0000, v188\n\t"
      "v_lshlrev_b32 v106, 16, v189\n\t"
      "v_and_b32 v107, 0xffff0000, v189\n\t"
      "v_lshlrev_b32 v108, 16, v190\n\t"
      "v_and_b32 v109, 0xffff0000, v190\n\t"
      "v_lshlrev_b32 v110, 16, v191\n\t"
      "v_and_b32 v111, 0xffff0000, v191\n\t"
      "v_mov_b64 v[48:49], v[96:97]\n\t"
      "v_mov_b64 v[50:51], v[98:99]\n\t"
      "v_mov_b64 v[52:53], v[100:101]\n\t"
      "v_mov_b64 v[54:55], v[102:103]\n\t"
      "v_mov_b64 v[56:57], v[104:105]\n\t"
      "v_mov_b64 v[58:59], v[106:107]\n\t"
      "v_mov_b64 v[60:61], v[108:109]\n\t"
      "v_mov_b64 v[62:63], v[110:111]\n\t"
      ::: "memory");
  if (wid == 0)
    asm volatile(
      "s_sleep 8\n\t"
      "buffer_load_dwordx4 v[220:223], v115, %[wss], 0 offen sc1\n\t"
      "buffer_load_dwordx4 v[224:227], v115, %[wss], 0 offen offset:2048 sc1\n\t"
      "buffer_load_dwordx4 v[228:231], v116, %[wss], 0 offen sc1\n\t"
        ::[wss] "s"(wss) : "memory");
  if (wid >= 2) {
#pragma unroll 1
    for (int i = 1 + wid; i < 8; i += 2) {
      const int jj = (i + mt) & 7, tw = jj >> 1, oh = jj & 1;
      asm volatile(
      "s_add_u32 m0, %[dst], 0\n\t"
      "s_nop 0\n\t"
      "buffer_load_dwordx4 v128, %[srd], %[so] offen offset:0 lds\n\t"
      "buffer_load_dwordx4 v129, %[srd], %[so] offen offset:1024 lds\n\t"
      "buffer_load_dwordx4 v130, %[srd], %[so] offen offset:2048 lds\n\t"
      "buffer_load_dwordx4 v131, %[srd], %[so] offen offset:3072 lds\n\t"
      "s_add_u32 m0, %[dst], 4096\n\t"
      "s_nop 0\n\t"
      "buffer_load_dwordx4 v132, %[srd], %[so] offen offset:0 lds\n\t"
      "buffer_load_dwordx4 v133, %[srd], %[so] offen offset:1024 lds\n\t"
      "buffer_load_dwordx4 v134, %[srd], %[so] offen offset:2048 lds\n\t"
      "buffer_load_dwordx4 v135, %[srd], %[so] offen offset:3072 lds\n\t"
      "s_add_u32 m0, %[dst], 8192\n\t"
      "s_nop 0\n\t"
      "buffer_load_dwordx4 v136, %[srd], %[so] offen offset:0 lds\n\t"
          ::[dst] "s"(lds0 + tw * WREG + oh * 9216), [srd] "s"(w1srd), [so] "s"(tw * 2304 + oh * 147456) : "memory", "scc");
    }
  }
  u32 hb = 0;
#pragma unroll 1
  for (int k = 0; k < 2; ++k) {
    if (k == 1) {
      asm volatile(
      "v_readfirstlane_b32 s64, v96\n\t"
      "v_mov_b32 v201, %[tag]\n\t"
      "v_mov_b32 v203, %[tag]\n\t"
      "s_xor_b32 s66, s64, 0x80000000\n\t"
      "s_mov_b32 s67, s66\n\t"
      "s_nop 0\n\t"
      "v_pk_add_f32 v[184:185], v[96:97], s[66:67]\n\t"
      "v_pk_add_f32 v[186:187], v[98:99], s[66:67]\n\t"
      "v_pk_add_f32 v[188:189], v[100:101], s[66:67]\n\t"
      "v_pk_add_f32 v[190:191], v[102:103], s[66:67]\n\t"
      "v_pk_add_f32 v[192:193], v[104:105], s[66:67]\n\t"
      "v_pk_add_f32 v[194:195], v[106:107], s[66:67]\n\t"
      "v_pk_add_f32 v[196:197], v[108:109], s[66:67]\n\t"
      "v_pk_add_f32 v[198:199], v[110:111], s[66:67]\n\t"
      "v_pk_add_f32 v[204:205], v[184:185], v[186:187]\n\t"
      "v_pk_add_f32 v[206:207], v[188:189], v[190:191]\n\t"
      "v_pk_mul_f32 v[208:209], v[184:185], v[184:185]\n\t"
      "v_pk_mul_f32 v[210:211], v[186:187], v[186:187]\n\t"
      "v_pk_add_f32 v[204:205], v[204:205], v[192:193]\n\t"
      "v_pk_add_f32 v[206:207], v[206:207], v[194:195]\n\t"
      "v_pk_fma_f32 v[208:209], v[188:189], v[188:189], v[208:209]\n\t"
      "v_pk_fma_f32 v[210:211], v[190:191], v[190:191], v[210:211]\n\t"
      "v_pk_add_f32 v[204:205], v[204:205], v[196:197]\n\t"
      "v_pk_add_f32 v[206:207], v[206:207], v[198:199]\n\t"
      "v_pk_fma_f32 v[208:209], v[192:193], v[192:193], v[208:209]\n\t"
      "v_pk_fma_f32 v[210:211], v[194:195], v[194:195], v[210:211]\n\t"
      "v_pk_add_f32 v[204:205], v[204:205], v[206:207]\n\t"
      "v_pk_fma_f32 v[208:209], v[196:197], v[196:197], v[208:209]\n\t"
      "v_pk_fma_f32 v[210:211], v[198:199], v[198:199], v[210:211]\n\t"
      "v_add_f32 v204, v204, v205\n\t"
      "v_pk_add_f32 v[208:209], v[208:209], v[210:211]\n\t"
      "v_add_f32 v208, v208, v209\n\t"
      "s_nop 0\n\t"
      "s_nop 0\n\t"
      "v_add_f32_dpp v204, v204, v204 quad_perm:[1,0,3,2] row_mask:0xf bank_mask:0xf\n\t"
      "v_add_f32_dpp v208, v208, v208 quad_perm:[1,0,3,2] row_mask:0xf bank_mask:0xf\n\t"
      "s_nop 0\n\t"
      "v_add_f32_dpp v204, v204, v204 quad_perm:[2,3,0,1] row_mask:0xf bank_mask:0xf\n\t"
      "v_add_f32_dpp v208, v208, v208 quad_perm:[2,3,0,1] row_mask:0xf bank_mask:0xf\n\t"
      "s_nop 0\n\t"
      "v_add_f32_dpp v204, v204, v204 row_half_mirror row_mask:0xf bank_mask:0xf\n\t"
      "v_add_f32_dpp v208, v208, v208 row_half_mirror row_mask:0xf bank_mask:0xf\n\t"
      "s_nop 0\n\t"
      "v_add_f32_dpp v204, v204, v204 row_mirror row_mask:0xf bank_mask:0xf\n\t"
      "v_add_f32_dpp v208, v208, v208 row_mirror row_mask:0xf bank_mask:0xf\n\t"
      "s_nop 0\n\t"
      "v_add_f32_dpp v204, v204, v204 row_bcast:15 row_mask:0xa bank_mask:0xf\n\t"
      "v_add_f32_dpp v208, v208, v208 row_bcast:15 row_mask:0xa bank_mask:0xf\n\t"
      "s_nop 0\n\t"
      "v_add_f32_dpp v204, v204, v204 row_bcast:31 row_mask:0xc bank_mask:0xf\n\t"
      "v_add_f32_dpp v208, v208, v208 row_bcast:31 row_mask:0xc bank_mask:0xf\n\t"
      "s_mov_b32 exec_lo, 0\n\t"
      "s_brev_b32 exec_hi, 1\n\t"
      "v_mul_f32 v205, 0x3a800000, v204\n\t"
      "v_fma_f32 v202, -v204, v205, v208\n\t"
      "v_add_f32 v200, s64, v205\n\t"
      "v_max_f32 v202, 0, v202\n\t"
      "ds_write_b128 v147, v[200:203] offset:21216\n\t"
      "s_mov_b64 exec, 0xff\n\t"
      "s_cmp_lg_u32 %[wid], 0\n\t"
      "s_waitcnt lgkmcnt(0)\n\t"
      "s_barrier\n\t"
      "s_cbranch_scc1 1f\n\t"
      "ds_read_b128 v[200:203], v119 offset:40656\n\t"
      "s_waitcnt lgkmcnt(0)\n\t"
      "buffer_store_dwordx4 v[200:203], v112, %[wss], %[so] offen sc1\n\t"
      "s_mov_b64 exec, -1\n\t"
      "s_sleep 8\n\t"
      "buffer_load_dwordx4 v[220:223], v115, %[wss], %[sp] offen sc1\n\t"
      "buffer_load_dwordx4 v[224:227], v115, %[wss], %[sp] offen offset:2048 sc1\n\t"
      "buffer_load_dwordx4 v[228:231], v116, %[wss], 0 offen sc1\n\t"
      "1:\n\t"
      "s_mov_b64 exec, -1\n\t"
          ::[tag] "s"(tag), [wid] "s"(wid), [wss] "s"(wss), [so] "s"(gbase + 32768 + hb + r * 128), [sp] "s"(32768 + hb)
          : "memory", "scc", "s64", "s66", "s67");
    }
    asm volatile(
      "ds_read2_b64 v[248:251], v117 offset1:4\n\t"
      "ds_read2_b64 v[252:255], v117 offset0:8 offset1:12\n\t"
      "s_mov_b32 s66, 0x100000\n\t"
      "s_cmp_lg_u32 %[wid], 0\n\t"
      "s_waitcnt lgkmcnt(0)\n\t"
      "v_add_u32 v117, 128, v117\n\t"
      "v_lshlrev_b32 v232, 16, v248\n\t"
      "v_and_b32 v233, 0xffff0000, v248\n\t"
      "v_lshlrev_b32 v234, 16, v249\n\t"
      "v_and_b32 v235, 0xffff0000, v249\n\t"
      "v_lshlrev_b32 v236, 16, v250\n\t"
      "v_and_b32 v237, 0xffff0000, v250\n\t"
      "v_lshlrev_b32 v238, 16, v251\n\t"
      "v_and_b32 v239, 0xffff0000, v251\n\t"
      "v_lshlrev_b32 v240, 16, v252\n\t"
      "v_and_b32 v241, 0xffff0000, v252\n\t"
      "v_lshlrev_b32 v242, 16, v253\n\t"
      "v_and_b32 v243, 0xffff0000, v253\n\t"
      "v_lshlrev_b32 v244, 16, v254\n\t"
      "v_and_b32 v245, 0xffff0000, v254\n\t"
      "v_lshlrev_b32 v246, 16, v255\n\t"
      "v_and_b32 v247, 0xffff0000, v255\n\t"
      "s_cbranch_scc1 3f\n\t"
      "1:\n\t"
      "s_waitcnt vmcnt(0)\n\t"
      "v_xor_b32 v221, %[tag], v221\n\t"
      "v_xor_b32 v223, %[tag], v223\n\t"
      "v_xor_b32 v225, %[tag], v225\n\t"
      "v_xor_b32 v227, %[tag], v227\n\t"
      "v_xor_b32 v229, %[tag], v229\n\t"
      "v_xor_b32 v231, %[tag], v231\n\t"
      "v_or3_b32 v221, v221, v223, v225\n\t"
      "v_or3_b32 v227, v227, v229, v231\n\t"
      "v_or_b32 v221, v221, v227\n\t"
      "v_cmp_ne_u32 vcc, 0, v221\n\t"
      "v_mov_b32_dpp v228, v228 quad_perm:[1,0,3,2] row_mask:0xf bank_mask:0xf\n\t"
      "v_sub_u32 v228, v230, v228\n\t"
      "s_cbranch_vccz 2f\n\t"
      "v_readlane_b32 s64, v228, 1\n\t"
      "v_readlane_b32 s65, v228, 3\n\t"
      "s_nop 3\n\t"
      "s_add_u32 s64, s64, s65\n\t"
      "s_lshr_b32 s64, s64, 18\n\t"
      "s_and_b32 s64, s64, 0x2000\n\t"
      "s_add_u32 s64, s64, %[so]\n\t"
      "buffer_load_dwordx4 v[220:223], v115, %[wss], s64 offen sc1\n\t"
      "buffer_load_dwordx4 v[224:227], v115, %[wss], s64 offen offset:2048 sc1\n\t"
      "buffer_load_dwordx4 v[228:231], v116, %[wss], 0 offen sc1\n\t"
      "s_sub_u32 s66, s66, 1\n\t"
      "s_cbranch_scc0 1b\n\t"
      "s_waitcnt vmcnt(0)\n\t"
      "2:\n\t"
      "v_readfirstlane_b32 s64, v220\n\t"
      "v_add_f32 v222, v222, v226\n\t"
      "s_nop 0\n\t"
      "v_subrev_f32 v220, s64, v220\n\t"
      "v_subrev_f32 v224, s64, v224\n\t"
      "v_mul_f32 v221, v220, v220\n\t"
      "v_add_f32 v220, v220, v224\n\t"
      "v_fmac_f32 v221, v224, v224\n\t"
      "v_fmac_f32 v222, 0x44800000, v221\n\t"
      "v_add_u32_dpp v228, v228, v228 quad_perm:[2,3,0,1] row_mask:0xf bank_mask:0xf\n\t"
      "s_nop 0\n\t"
      "v_add_f32_dpp v220, v220, v220 quad_perm:[1,0,3,2] row_mask:0xf bank_mask:0xf\n\t"
      "v_add_f32_dpp v222, v222, v222 quad_perm:[1,0,3,2] row_mask:0xf bank_mask:0xf\n\t"
      "s_nop 0\n\t"
      "v_add_f32_dpp v220, v220, v220 quad_perm:[2,3,0,1] row_mask:0xf bank_mask:0xf\n\t"
      "v_add_f32_dpp v222, v222, v222 quad_perm:[2,3,0,1] row_mask:0xf bank_mask:0xf\n\t"
      "s_nop 0\n\t"
      "v_add_f32_dpp v220, v220, v220 row_half_mirror row_mask:0xf bank_mask:0xf\n\t"
      "v_add_f32_dpp v222, v222, v222 row_half_mirror row_mask:0xf bank_mask:0xf\n\t"
      "s_nop 0\n\t"
      "v_add_f32_dpp v220, v220, v220 row_mirror row_mask:0xf bank_mask:0xf\n\t"
      "v_add_f32_dpp v222, v222, v222 row_mirror row_mask:0xf bank_mask:0xf\n\t"
      "s_nop 0\n\t"
      "v_add_f32_dpp v220, v220, v220 row_bcast:15 row_mask:0xa bank_mask:0xf\n\t"
      "v_add_f32_dpp v222, v222, v222 row_bcast:15 row_mask:0xa bank_mask:0xf\n\t"
      "s_nop 0\n\t"
      "v_add_f32_dpp v220, v220, v220 row_bcast:31 row_mask:0xc bank_mask:0xf\n\t"
      "v_add_f32_dpp v222, v222, v222 row_bcast:31 row_mask:0xc bank_mask:0xf\n\t"
      "s_mov_b32 exec_lo, 0\n\t"
      "s_brev_b32 exec_hi, 1\n\t"
      "v_lshrrev_b32 v228, 18, v228\n\t"
      "v_mul_f32 v220, 0x3c000000, v220\n\t"
      "v_mul_f32 v222, 0x37000000, v222\n\t"
      "v_fma_f32 v222, -v220, v220, v222\n\t"
      "v_max_f32 v222, 0, v222\n\t"
      "v_add_f32 v222, 0x3727c5ac, v222\n\t"
      "v_rsq_f32 v221, v222\n\t"
      "v_add_f32 v220, s64, v220\n\t"
      "v_and_b32 v222, 0x2000, v228\n\t"
      "ds_write_b96 v118, v[220:222] offset:40640\n\t"
      "s_mov_b64 exec, -1\n\t"
      "s_waitcnt lgkmcnt(0)\n\t"
      "3:\n\t"
        ::[tag] "s"(tag), [wid] "s"(wid), [wss] "s"(wss), [so] "s"(k * 32768) : "memory", "vcc", "scc", "s64", "s65", "s66");
    __builtin_amdgcn_s_barrier();
    const u32 actb = WS_ACT + b * ((4u << 20) - WS_IMG) + k * ACT_STRIDE;
    asm volatile(
      "ds_read_b96 v[248:250], v118 offset:40640\n\t"
      "s_mov_b32 s64, 0xbfb8aa3b\n\t"
      "s_mov_b32 s65, 0xbfb8aa3b\n\t"
      "s_waitcnt lgkmcnt(0)\n\t"
      "v_readfirstlane_b32 %[hb], v250\n\t"
      "v_pk_mul_f32 v[232:233], v[248:249], v[232:233] op_sel:[1,0]\n\t"
      "v_pk_mul_f32 v[234:235], v[248:249], v[234:235] op_sel:[1,0]\n\t"
      "v_pk_mul_f32 v[236:237], v[248:249], v[236:237] op_sel:[1,0]\n\t"
      "v_pk_mul_f32 v[238:239], v[248:249], v[238:239] op_sel:[1,0]\n\t"
      "v_pk_add_f32 v[184:185], v[96:97], v[248:249] op_sel_hi:[1,0] neg_lo:[0,1] neg_hi:[0,1]\n\t"
      "v_pk_add_f32 v[186:187], v[98:99], v[248:249] op_sel_hi:[1,0] neg_lo:[0,1] neg_hi:[0,1]\n\t"
      "v_pk_add_f32 v[188:189], v[100:101], v[248:249] op_sel_hi:[1,0] neg_lo:[0,1] neg_hi:[0,1]\n\t"
      "v_pk_add_f32 v[190:191], v[102:103], v[248:249] op_sel_hi:[1,0] neg_lo:[0,1] neg_hi:[0,1]\n\t"
      "v_pk_add_f32 v[192:193], v[104:105], v[248:249] op_sel_hi:[1,0] neg_lo:[0,1] neg_hi:[0,1]\n\t"
      "v_pk_add_f32 v[194:195], v[106:107], v[248:249] op_sel_hi:[1,0] neg_lo:[0,1] neg_hi:[0,1]\n\t"
      "v_pk_add_f32 v[196:197], v[108:109], v[248:249] op_sel_hi:[1,0] neg_lo:[0,1] neg_hi:[0,1]\n\t"
      "v_pk_add_f32 v[198:199], v[110:111], v[248:249] op_sel_hi:[1,0] neg_lo:[0,1] neg_hi:[0,1]\n\t"
      "v_pk_fma_f32 v[184:185], v[184:185], v[232:233], v[240:241]\n\t"
      "v_pk_fma_f32 v[186:187], v[186:187], v[234:235], v[242:243]\n\t"
      "v_pk_fma_f32 v[188:189], v[188:189], v[236:237], v[244:245]\n\t"
      "v_pk_fma_f32 v[190:191], v[190:191], v[238:239], v[246:247]\n\t"
      "v_pk_fma_f32 v[192:193], v[192:193], v[232:233], v[240:241]\n\t"
      "v_pk_fma_f32 v[194:195], v[194:195], v[234:235], v[242:243]\n\t"
      "v_pk_fma_f32 v[196:197], v[196:197], v[236:237], v[244:245]\n\t"
      "v_pk_fma_f32 v[198:199], v[198:199], v[238:239], v[246:247]\n\t"
      "v_pk_mul_f32 v[200:201], v[184:185], s[64:65]\n\t"
      "v_pk_mul_f32 v[202:203], v[186:187], s[64:65]\n\t"
      "v_pk_mul_f32 v[204:205], v[188:189], s[64:65]\n\t"
      "v_pk_mul_f32 v[206:207], v[190:191], s[64:65]\n\t"
      "v_pk_mul_f32 v[208:209], v[192:193], s[64:65]\n\t"
      "v_pk_mul_f32 v[210:211], v[194:195], s[64:65]\n\t"
      "v_pk_mul_f32 v[212:213], v[196:197], s[64:65]\n\t"
      "v_pk_mul_f32 v[214:215], v[198:199], s[64:65]\n\t"
      "s_add_u32 s66, %[so], %[hb]\n\t"
      "v_exp_f32 v200, v200\n\t"
      "v_exp_f32 v201, v201\n\t"
      "v_exp_f32 v202, v202\n\t"
      "v_exp_f32 v203, v203\n\t"
      "v_exp_f32 v204, v204\n\t"
      "v_exp_f32 v205, v205\n\t"
      "v_exp_f32 v206, v206\n\t"
      "v_exp_f32 v207, v207\n\t"
      "v_exp_f32 v208, v208\n\t"
      "v_exp_f32 v209, v209\n\t"
      "v_exp_f32 v210, v210\n\t"
      "v_exp_f32 v211, v211\n\t"
      "v_exp_f32 v212, v212\n\t"
      "v_exp_f32 v213, v213\n\t"
      "v_exp_f32 v214, v214\n\t"
      "v_exp_f32 v215, v215\n\t"
      "v_pk_add_f32 v[200:201], v[200:201], 1.0 op_sel_hi:[1,0]\n\t"
      "v_pk_add_f32 v[202:203], v[202:203], 1.0 op_sel_hi:[1,0]\n\t"
      "v_pk_add_f32 v[204:205], v[204:205], 1.0 op_sel_hi:[1,0]\n\t"
      "v_pk_add_f32 v[206:207], v[206:207], 1.0 op_sel_hi:[1,0]\n\t"
      "v_pk_add_f32 v[208:209], v[208:209], 1.0 op_sel_hi:[1,0]\n\t"
      "v_pk_add_f32 v[210:211], v[210:211], 1.0 op_sel_hi:[1,0]\n\t"
      "v_pk_add_f32 v[212:213], v[212:213], 1.0 op_sel_hi:[1,0]\n\t"
      "v_pk_add_f32 v[214:215], v[214:215], 1.0 op_sel_hi:[1,0]\n\t"
      "v_rcp_f32 v200, v200\n\t"
      "v_rcp_f32 v201, v201\n\t"
      "v_rcp_f32 v202, v202\n\t"
      "v_rcp_f32 v203, v203\n\t"
      "v_rcp_f32 v204, v204\n\t"
      "v_rcp_f32 v205, v205\n\t"
      "v_rcp_f32 v206, v206\n\t"
      "v_rcp_f32 v207, v207\n\t"
      "v_pk_mul_f32 v[184:185], v[184:185], v[200:201]\n\t"
      "v_pk_mul_f32 v[186:187], v[186:187], v[202:203]\n\t"
      "v_pk_mul_f32 v[188:189], v[188:189], v[204:205]\n\t"
      "v_pk_mul_f32 v[190:191], v[190:191], v[206:207]\n\t"
      "v_cvt_pk_bf16_f32 v216, v184, v185\n\t"
      "v_cvt_pk_bf16_f32 v217, v186, v187\n\t"
      "v_cvt_pk_bf16_f32 v218, v188, v189\n\t"
      "v_cvt_pk_bf16_f32 v219, v190, v191\n\t"
      "ds_write_b64 v120, v[216:217] offset:0\n\t"
      "ds_write_b64 v121, v[218:219] offset:0\n\t"
      "ds_read_b128 v[224:227], v147 offset:10240\n\t"
      "v_rcp_f32 v208, v208\n\t"
      "v_rcp_f32 v209, v209\n\t"
      "v_rcp_f32 v210, v210\n\t"
      "v_rcp_f32 v211, v211\n\t"
      "v_rcp_f32 v212, v212\n\t"
      "v_rcp_f32 v213, v213\n\t"
      "v_rcp_f32 v214, v214\n\t"
      "v_rcp_f32 v215, v215\n\t"
      "v_pk_mul_f32 v[192:193], v[192:193], v[208:209]\n\t"
      "v_pk_mul_f32 v[194:195], v[194:195], v[210:211]\n\t"
      "v_pk_mul_f32 v[196:197], v[196:197], v[212:213]\n\t"
      "v_pk_mul_f32 v[198:199], v[198:199], v[214:215]\n\t"
      "v_cvt_pk_bf16_f32 v220, v192, v193\n\t"
      "v_cvt_pk_bf16_f32 v221, v194, v195\n\t"
      "v_cvt_pk_bf16_f32 v222, v196, v197\n\t"
      "v_cvt_pk_bf16_f32 v223, v198, v199\n\t"
      "ds_write_b64 v120, v[220:221] offset:1024\n\t"
      "ds_write_b64 v121, v[222:223] offset:1024\n\t"
      "ds_read_b128 v[228:231], v147 offset:11264\n\t"
      "s_waitcnt lgkmcnt(3)\n\t"
      "buffer_store_dwordx4 v[224:227], v112, %[wss], s66 offen sc1\n\t"
      "s_waitcnt lgkmcnt(0)\n\t"
      "buffer_store_dwordx4 v[228:231], v112, %[wss], s66 offen offset:1024 sc1\n\t"
      "ds_write_b128 v123, v[76:79]\n\t"
        : [hb] "=&s"(hb)
        : [wss] "s"(wss), [so] "s"(actb + (nt * 8 + mt) * 16384 + wid * 2048)
        : "memory", "scc", "s64", "s65", "s66");
    if (wid == 1) {
      if (k == 0)
        asm volatile(
      "s_mov_b32 s64, 0x100000\n\t"
      "1:\n\t"
      "buffer_load_dwordx4 v[184:187], v122, %[wss], %[so] offen offset:0 sc1\n\t"
      "buffer_load_dwordx4 v[188:191], v122, %[wss], %[so] offen offset:16 sc1\n\t"
      "buffer_load_dwordx4 v[192:195], v122, %[wss], %[so] offen offset:128 sc1\n\t"
      "buffer_load_dwordx4 v[196:199], v122, %[wss], %[so] offen offset:144 sc1\n\t"
      "s_waitcnt vmcnt(0)\n\t"
      "v_xor_b32 v185, %[tag], v185\n\t"
      "v_xor_b32 v187, %[tag], v187\n\t"
      "v_xor_b32 v189, %[tag], v189\n\t"
      "v_xor_b32 v191, %[tag], v191\n\t"
      "v_xor_b32 v193, %[tag], v193\n\t"
      "v_xor_b32 v195, %[tag], v195\n\t"
      "v_xor_b32 v197, %[tag], v197\n\t"
      "v_xor_b32 v199, %[tag], v199\n\t"
      "v_or3_b32 v185, v185, v187, v189\n\t"
      "v_or3_b32 v191, v191, v193, v195\n\t"
      "v_or3_b32 v185, v185, v197, v199\n\t"
      "v_or_b32 v185, v185, v191\n\t"
      "v_cmp_ne_u32 vcc, 0, v185\n\t"
      "s_cbranch_vccz 2f\n\t"
      "s_sub_u32 s64, s64, 1\n\t"
      "s_cbranch_scc0 1b\n\t"
      "2:\n\t"
            ::[tag] "s"(tag), [wss] "s"(wss), [so] "s"(WS_EMB + g * 16384 + cc * 2048 + hb) : "memory", "vcc", "scc", "s64");
      else if (g == 0 && r == 0)
        asm volatile("v_mov_b32 v64, %0\n\tbuffer_store_dword v64, off, %1, 0 sc1" ::"s"(tag), "s"(wss) : "memory");
    }
    u32x4 asrd = make_srd(ws + b * WS_IMG + actb + (32 * wid + mt - 1) * 16384 + hb, 3 * 131072 + 34816);
    u64 wc = (u64)((k ? w2 : w1) + (size_t)o0 * 9216), wn = (u64)(w2 + (size_t)o0 * 9216);
    u32 wn2 = k ? 0u : 32u * 9216u;
    const u32 wq = wid * 2304;
    asm volatile("" : "+s"(asrd), "+s"(wc), "+s"(wn2));
    const u32 fso = WS_FLAG + k * 16384 + hb;
    asm volatile(
      "v_mov_b32 v65, 0\n\t"
      "v_mov_b32 v66, 0\n\t"
      "v_mov_b32 v67, 0\n\t"
      "v_mov_b32 v68, 0\n\t"
      "v_mov_b32 v69, 0\n\t"
      "v_mov_b32 v70, 0\n\t"
      "v_mov_b32 v71, 0\n\t"
      "v_mov_b32 v200, %[tag]\n\t"
      "v_mov_b32 v201, %[tag]\n\t"
      "v_mov_b32 v202, %[tag]\n\t"
      "v_mov_b32 v203, %[tag]\n\t"
      "s_cmp_lg_u32 %[wid], 0\n\t"
      "s_waitcnt vmcnt(0) lgkmcnt(0)\n\t"
      "s_barrier\n\t"
      "s_cbranch_scc1 1f\n\t"
      "s_mov_b64 exec, 15\n\t"
      "buffer_store_dwordx4 v[200:203], v112, %[wss], %[fs] offen sc1\n\t"
      "s_mov_b64 exec, -1\n\t"
      "1:\n\t"
      "v_mov_b32 v64, 0\n\t"
      "s_mov_b32 s76, 0x07060100\n\t"
      "s_mov_b32 s77, 0x05040302\n\t"
      "ds_read_b128 v[220:223], v89 offset:0\n\t"
      "ds_read_b128 v[224:227], v90 offset:16\n\t"
      "ds_read_b128 v[228:231], v91 offset:32\n\t"
      "ds_read_b128 v[232:235], v92 offset:48\n\t"
      "ds_read_b128 v[236:239], v89 offset:64\n\t"
      "ds_read_b128 v[240:243], v90 offset:80\n\t"
      "ds_read_b128 v[244:247], v91 offset:96\n\t"
      "ds_read_b128 v[248:251], v92 offset:112\n\t"
      "ds_read_b128 v[252:255], v89 offset:128\n\t"
      "s_waitcnt lgkmcnt(0)\n\t"
      "v_perm_b32 v148, v224, v220, s76\n\t"
      "v_perm_b32 v152, v225, v220, s77\n\t"
      "v_perm_b32 v156, v225, v221, s76\n\t"
      "v_perm_b32 v160, v226, v221, s77\n\t"
      "v_perm_b32 v164, v226, v222, s76\n\t"
      "v_perm_b32 v168, v227, v222, s77\n\t"
      "v_perm_b32 v172, v227, v223, s76\n\t"
      "v_perm_b32 v176, v228, v223, s77\n\t"
      "v_perm_b32 v180, v228, v224, s76\n\t"
      "v_perm_b32 v149, v233, v229, s76\n\t"
      "v_perm_b32 v153, v234, v229, s77\n\t"
      "v_perm_b32 v157, v234, v230, s76\n\t"
      "v_perm_b32 v161, v235, v230, s77\n\t"
      "v_perm_b32 v165, v235, v231, s76\n\t"
      "v_perm_b32 v169, v236, v231, s77\n\t"
      "v_perm_b32 v173, v236, v232, s76\n\t"
      "v_perm_b32 v177, v237, v232, s77\n\t"
      "v_perm_b32 v181, v237, v233, s76\n\t"
      "v_perm_b32 v150, v242, v238, s76\n\t"
      "v_perm_b32 v154, v243, v238, s77\n\t"
      "v_perm_b32 v158, v243, v239, s76\n\t"
      "v_perm_b32 v162, v244, v239, s77\n\t"
      "v_perm_b32 v166, v244, v240, s76\n\t"
      "v_perm_b32 v170, v245, v240, s77\n\t"
      "v_perm_b32 v174, v245, v241, s76\n\t"
      "v_perm_b32 v178, v246, v241, s77\n\t"
      "v_perm_b32 v182, v246, v242, s76\n\t"
      "v_perm_b32 v151, v251, v247, s76\n\t"
      "v_perm_b32 v155, v252, v247, s77\n\t"
      "v_perm_b32 v159, v252, v248, s76\n\t"
      "v_perm_b32 v163, v253, v248, s77\n\t"
      "v_perm_b32 v167, v253, v249, s76\n\t"
      "v_perm_b32 v171, v254, v249, s77\n\t"
      "v_perm_b32 v175, v254, v250, s76\n\t"
      "v_perm_b32 v179, v255, v250, s77\n\t"
      "v_perm_b32 v183, v255, v251, s76\n\t"
      "s_cmp_lg_u32 %[k], 0\n\t"
      "s_cbranch_scc1 2f\n\t"
      "s_cmp_lg_u32 %[wid], 1\n\t"
      "s_cbranch_scc1 2f\n\t"
      "v_add_f32_dpp v184, v184, v184 quad_perm:[1,0,3,2] row_mask:0xf bank_mask:0xf\n\t"
      "v_add_f32_dpp v186, v186, v186 quad_perm:[1,0,3,2] row_mask:0xf bank_mask:0xf\n\t"
      "v_add_f32_dpp v188, v188, v188 quad_perm:[1,0,3,2] row_mask:0xf bank_mask:0xf\n\t"
      "v_add_f32_dpp v190, v190, v190 quad_perm:[1,0,3,2] row_mask:0xf bank_mask:0xf\n\t"
      "v_add_f32_dpp v192, v192, v192 quad_perm:[1,0,3,2] row_mask:0xf bank_mask:0xf\n\t"
      "v_add_f32_dpp v194, v194, v194 quad_perm:[1,0,3,2] row_mask:0xf bank_mask:0xf\n\t"
      "v_add_f32_dpp v196, v196, v196 quad_perm:[1,0,3,2] row_mask:0xf bank_mask:0xf\n\t"
      "v_add_f32_dpp v198, v198, v198 quad_perm:[1,0,3,2] row_mask:0xf bank_mask:0xf\n\t"
      "v_add_f32_dpp v184, v184, v184 quad_perm:[2,3,0,1] row_mask:0xf bank_mask:0xf\n\t"
      "v_add_f32_dpp v186, v186, v186 quad_perm:[2,3,0,1] row_mask:0xf bank_mask:0xf\n\t"
      "v_add_f32_dpp v188, v188, v188 quad_perm:[2,3,0,1] row_mask:0xf bank_mask:0xf\n\t"
      "v_add_f32_dpp v190, v190, v190 quad_perm:[2,3,0,1] row_mask:0xf bank_mask:0xf\n\t"
      "v_add_f32_dpp v192, v192, v192 quad_perm:[2,3,0,1] row_mask:0xf bank_mask:0xf\n\t"
      "v_add_f32_dpp v194, v194, v194 quad_perm:[2,3,0,1] row_mask:0xf bank_mask:0xf\n\t"
      "v_add_f32_dpp v196, v196, v196 quad_perm:[2,3,0,1] row_mask:0xf bank_mask:0xf\n\t"
      "v_add_f32_dpp v198, v198, v198 quad_perm:[2,3,0,1] row_mask:0xf bank_mask:0xf\n\t"
      "v_add_f32_dpp v64, v184, v184 row_half_mirror row_mask:0xf bank_mask:0xf\n\t"
      "v_add_f32_dpp v65, v186, v186 row_half_mirror row_mask:0xf bank_mask:0xf\n\t"
      "v_add_f32_dpp v66, v188, v188 row_half_mirror row_mask:0xf bank_mask:0xf\n\t"
      "v_add_f32_dpp v67, v190, v190 row_half_mirror row_mask:0xf bank_mask:0xf\n\t"
      "v_add_f32_dpp v68, v192, v192 row_half_mirror row_mask:0xf bank_mask:0xf\n\t"
      "v_add_f32_dpp v69, v194, v194 row_half_mirror row_mask:0xf bank_mask:0xf\n\t"
      "v_add_f32_dpp v70, v196, v196 row_half_mirror row_mask:0xf bank_mask:0xf\n\t"
      "v_add_f32_dpp v71, v198, v198 row_half_mirror row_mask:0xf bank_mask:0xf\n\t"
      "2:\n\t"
      "v_accvgpr_write_b32 a0, v64\n\t"
      "v_accvgpr_write_b32 a1, v65\n\t"
      "v_accvgpr_write_b32 a2, v66\n\t"
      "v_accvgpr_write_b32 a3, v67\n\t"
      "v_accvgpr_write_b32 a4, v68\n\t"
      "v_accvgpr_write_b32 a5, v69\n\t"
      "v_accvgpr_write_b32 a6, v70\n\t"
      "v_accvgpr_write_b32 a7, v71\n\t"
      "v_accvgpr_write_b32 a8, v64\n\t"
      "v_accvgpr_write_b32 a9, v65\n\t"
      "v_accvgpr_write_b32 a10, v66\n\t"
      "v_accvgpr_write_b32 a11, v67\n\t"
      "v_accvgpr_write_b32 a12, v68\n\t"
      "v_accvgpr_write_b32 a13, v69\n\t"
      "v_accvgpr_write_b32 a14, v70\n\t"
      "v_accvgpr_write_b32 a15, v71\n\t"
      "v_accvgpr_write_b32 a16, v64\n\t"
      "v_accvgpr_write_b32 a17, v65\n\t"
      "v_accvgpr_write_b32 a18, v66\n\t"
      "v_accvgpr_write_b32 a19, v67\n\t"
      "v_accvgpr_write_b32 a20, v68\n\t"
      "v_accvgpr_write_b32 a21, v69\n\t"
      "v_accvgpr_write_b32 a22, v70\n\t"
      "v_accvgpr_write_b32 a23, v71\n\t"
      "v_accvgpr_write_b32 a24, v64\n\t"
      "v_accvgpr_write_b32 a25, v65\n\t"
      "v_accvgpr_write_b32 a26, v66\n\t"
      "v_accvgpr_write_b32 a27, v67\n\t"
      "v_accvgpr_write_b32 a28, v68\n\t"
      "v_accvgpr_write_b32 a29, v69\n\t"
      "v_accvgpr_write_b32 a30, v70\n\t"
      "v_accvgpr_write_b32 a31, v71\n\t"
      "v_accvgpr_write_b32 a32, v64\n\t"
      "v_accvgpr_write_b32 a33, v65\n\t"
      "v_accvgpr_write_b32 a34, v66\n\t"
      "v_accvgpr_write_b32 a35, v67\n\t"
      "v_accvgpr_write_b32 a36, v68\n\t"
      "v_accvgpr_write_b32 a37, v69\n\t"
      "v_accvgpr_write_b32 a38, v70\n\t"
      "v_accvgpr_write_b32 a39, v71\n\t"
      "v_accvgpr_write_b32 a40, v64\n\t"
      "v_accvgpr_write_b32 a41, v65\n\t"
      "v_accvgpr_write_b32 a42, v66\n\t"
      "v_accvgpr_write_b32 a43, v67\n\t"
      "v_accvgpr_write_b32 a44, v68\n\t"
      "v_accvgpr_write_b32 a45, v69\n\t"
      "v_accvgpr_write_b32 a46, v70\n\t"
      "v_accvgpr_write_b32 a47, v71\n\t"
      "v_accvgpr_write_b32 a48, v64\n\t"
      "v_accvgpr_write_b32 a49, v65\n\t"
      "v_accvgpr_write_b32 a50, v66\n\t"
      "v_accvgpr_write_b32 a51, v67\n\t"
      "v_accvgpr_write_b32 a52, v68\n\t"
      "v_accvgpr_write_b32 a53, v69\n\t"
      "v_accvgpr_write_b32 a54, v70\n\t"
      "v_accvgpr_write_b32 a55, v71\n\t"
      "v_accvgpr_write_b32 a56, v64\n\t"
      "v_accvgpr_write_b32 a57, v65\n\t"
      "v_accvgpr_write_b32 a58, v66\n\t"
      "v_accvgpr_write_b32 a59, v67\n\t"
      "v_accvgpr_write_b32 a60, v68\n\t"
      "v_accvgpr_write_b32 a61, v69\n\t"
      "v_accvgpr_write_b32 a62, v70\n\t"
      "v_accvgpr_write_b32 a63, v71\n\t"
      "s_cmp_lg_u32 %[wid], 0\n\t"
      "s_cbranch_scc1 4f\n\t"
      "s_mov_b32 s64, 0x100000\n\t"
      "3:\n\t"
      "buffer_load_dword v204, v124, %[wss], %[fso] offen sc1\n\t"
      "s_waitcnt vmcnt(0)\n\t"
      "v_cmp_ne_u32 vcc, %[tag], v204\n\t"
      "s_cbranch_vccz 4f\n\t"
      "s_sub_u32 s64, s64, 1\n\t"
      "s_cbranch_scc0 3b\n\t"
      "4:\n\t"
      "s_barrier\n\t"
        ::[tag] "s"(tag), [wid] "s"(wid), [k] "s"(k), [wss] "s"(wss), [fs] "s"(fso + (mt * 16 + nt) * 64), [fso] "s"(fso)
        : "memory", "vcc", "scc", "s64", "s76", "s77", "a0", "a1", "a2", "a3", "a4", "a5", "a6", "a7", "a8", "a9", "a10", "a11", "a12", "a13", "a14", "a15", "a16", "a17", "a18", "a19", "a20", "a21", "a22", "a23", "a24", "a25", "a26", "a27", "a28", "a29", "a30", "a31", "a32", "a33", "a34", "a35", "a36", "a37", "a38", "a39", "a40", "a41", "a42", "a43", "a44", "a45", "a46", "a47", "a48", "a49", "a50", "a51", "a52", "a53", "a54", "a55", "a56", "a57", "a58", "a59", "a60", "a61", "a62", "a63", "a64", "a65", "a66", "a67", "a68", "a69", "a70", "a71", "a72", "a73", "a74", "a75", "a76", "a77", "a78", "a79", "a80", "a81", "a82", "a83", "a84", "a85", "a86", "a87", "a88", "a89", "a90", "a91", "a92", "a93", "a94", "a95", "a96", "a97", "a98", "a99", "a100", "a101", "a102", "a103", "a104", "a105", "a106", "a107", "a108", "a109", "a110", "a111", "a112", "a113", "a114", "a115", "a116", "a117", "a118", "a119", "a120", "a121", "a122", "a123", "a124", "a125", "a126", "a127", "a128", "a129", "a130", "a131", "a132", "a133", "a134", "a135", "a136", "a137", "a138", "a139", "a140", "a141", "a142", "a143", "a144", "a145", "a146", "a147", "a148", "a149", "a150", "a151", "a152", "a153", "a154", "a155", "a156", "a157", "a158", "a159", "a160", "a161", "a162", "a163", "a164", "a165", "a166", "a167", "a168", "a169", "a170", "a171", "a172", "a173", "a174", "a175", "a176", "a177", "a178", "a179", "a180", "a181", "a182", "a183", "a184", "a185", "a186", "a187", "a188", "a189", "a190", "a191", "a192", "a193", "a194", "a195", "a196", "a197", "a198", "a199", "a200", "a201", "a202", "a203", "a204", "a205", "a206", "a207");
    {
      asm volatile(
      "s_add_u32 m0, %[wb], 18496\n\t"
      "s_nop 0\n\t"
      "buffer_load_dwordx4 v137, %[asrd], %[a0] offen sc1 lds\n\t"
      "buffer_load_dwordx4 v137, %[asrd], %[a0] offen offset:1024 sc1 lds\n\t"
      "s_add_u32 m0, %[wb], 20608\n\t"
      "s_nop 0\n\t"
      "buffer_load_dwordx4 v138, %[asrd], %[a0] offen sc1 lds\n\t"
      "buffer_load_dwordx4 v138, %[asrd], %[a0] offen offset:1024 sc1 lds\n\t"
      "s_add_u32 m0, %[wb], 22720\n\t"
      "s_nop 0\n\t"
      "buffer_load_dwordx4 v139, %[asrd], %[a0] offen sc1 lds\n\t"
      "buffer_load_dwordx4 v139, %[asrd], %[a0] offen offset:1024 sc1 lds\n\t"
      "s_add_u32 m0, %[wb], 24832\n\t"
      "s_nop 0\n\t"
      "buffer_load_dwordx4 v140, %[asrd], %[a0] offen sc1 lds\n\t"
      "buffer_load_dwordx4 v140, %[asrd], %[a0] offen offset:1024 sc1 lds\n\t"
      "s_add_u32 m0, %[wb], 26944\n\t"
      "s_nop 0\n\t"
      "buffer_load_dwordx4 v141, %[asrd], %[a0] offen sc1 lds\n\t"
      "buffer_load_dwordx4 v141, %[asrd], %[a0] offen offset:1024 sc1 lds\n\t"
      "s_add_u32 m0, %[wb], 29056\n\t"
      "s_nop 0\n\t"
      "buffer_load_dwordx4 v142, %[asrd], %[a0] offen sc1 lds\n\t"
      "buffer_load_dwordx4 v142, %[asrd], %[a0] offen offset:1024 sc1 lds\n\t"
      "s_mov_b64 s[68:69], %[wc01]\n\t"
      "s_mov_b32 s70, 0x48000\n\t"
      "s_mov_b32 s71, 0x20000\n\t"
      "s_mov_b32 s67, %[seq]\n\t"
      "s_mov_b64 vcc, 0\n\t"
      "s_and_b32 s78, s67, 3\n\t"
      "s_mul_i32 s73, s78, 576\n\t"
      "s_add_u32 s73, s73, %[wq]\n\t"
      "s_add_u32 s72, s73, 147456\n\t"
      "s_lshl_b32 s74, s78, 17\n\t"
      "s_mov_b32 s75, 0\n\t"
      "s_mov_b32 s76, 0x07060100\n\t"
      "s_mov_b32 s77, 0x05040302\n\t"
      "s_waitcnt vmcnt(10)\n\t"
      "ds_read_b128 a[64:67], v143 offset:0\n\t"
      "ds_read_b128 a[68:71], v144 offset:0\n\t"
      "ds_read_b128 a[72:75], v145 offset:0\n\t"
      "s_waitcnt vmcnt(8)\n\t"
      "ds_read_b128 a[88:91], v143 offset:2112\n\t"
      "ds_read_b128 a[92:95], v144 offset:2112\n\t"
      "ds_read_b128 a[96:99], v145 offset:2112\n\t"
      "s_waitcnt vmcnt(6)\n\t"
      "ds_read_b128 a[112:115], v143 offset:4224\n\t"
      "ds_read_b128 a[116:119], v144 offset:4224\n\t"
      "ds_read_b128 a[120:123], v145 offset:4224\n\t"
      "ds_read_b128 a[76:79], v143 offset:1024\n\t"
      "ds_read_b128 a[80:83], v144 offset:1024\n\t"
      "ds_read_b128 a[84:87], v145 offset:1024\n\t"
      "ds_read_b128 a[100:103], v143 offset:3136\n\t"
      "ds_read_b128 a[104:107], v144 offset:3136\n\t"
      "ds_read_b128 a[108:111], v145 offset:3136\n\t"
      "ds_read_b128 a[124:127], v143 offset:5248\n\t"
      "ds_read_b128 a[128:131], v144 offset:5248\n\t"
      "ds_read_b128 a[132:135], v145 offset:5248\n\t"
      "s_waitcnt vmcnt(4) lgkmcnt(9)\n\t"
      "1:\n\t"
      "s_waitcnt vmcnt(13)\n\t"
      "v_mfma_f32_16x16x32_bf16 a[0:3], v[148:151], a[64:67], a[0:3]\n\t"
      "ds_read_b128 a[136:139], v143 offset:6336\n\t"
      "s_add_u32 m0, %[wb], 0\n\t"
      "v_mfma_f32_16x16x32_bf16 a[0:3], v[152:155], a[68:71], a[0:3]\n\t"
      "buffer_load_dwordx4 v128, s[68:71], s73 offen offset:0 lds\n\t"
      "v_mfma_f32_16x16x32_bf16 a[0:3], v[156:159], a[72:75], a[0:3]\n\t"
      "ds_read_b128 a[140:143], v144 offset:6336\n\t"
      "v_mfma_f32_16x16x32_bf16 a[0:3], v[160:163], a[88:91], a[0:3]\n\t"
      "ds_read_b128 a[144:147], v145 offset:6336\n\t"
      "v_mfma_f32_16x16x32_bf16 a[0:3], v[164:167], a[92:95], a[0:3]\n\t"
      "ds_read_b128 a[148:151], v143 offset:7360\n\t"
      "v_mfma_f32_16x16x32_bf16 a[0:3], v[168:171], a[96:99], a[0:3]\n\t"
      "v_mfma_f32_16x16x32_bf16 a[0:3], v[172:175], a[112:115], a[0:3]\n\t"
      "buffer_load_dwordx4 v129, s[68:71], s73 offen offset:1024 lds\n\t"
      "v_mfma_f32_16x16x32_bf16 a[0:3], v[176:179], a[116:119], a[0:3]\n\t"
      "ds_read_b128 a[152:155], v144 offset:7360\n\t"
      "v_mfma_f32_16x16x32_bf16 a[0:3], v[180:183], a[120:123], a[0:3]\n\t"
      "s_waitcnt lgkmcnt(4)\n\t"
      "ds_read_b128 a[156:159], v145 offset:7360\n\t"
      "v_mfma_f32_16x16x32_bf16 a[8:11], v[148:151], a[76:79], a[8:11]\n\t"
      "v_mfma_f32_16x16x32_bf16 a[8:11], v[152:155], a[80:83], a[8:11]\n\t"
      "s_waitcnt vmcnt(6)\n\t"
      "v_mfma_f32_16x16x32_bf16 a[8:11], v[156:159], a[84:87], a[8:11]\n\t"
      "buffer_load_dwordx4 v130, s[68:71], s73 offen offset:2048 lds\n\t"
      "v_mfma_f32_16x16x32_bf16 a[8:11], v[160:163], a[100:103], a[8:11]\n\t"
      "ds_read_b128 v[220:223], v89 offset:9216\n\t"
      "v_mfma_f32_16x16x32_bf16 a[8:11], v[164:167], a[104:107], a[8:11]\n\t"
      "ds_read_b128 v[224:227], v90 offset:9232\n\t"
      "v_mfma_f32_16x16x32_bf16 a[8:11], v[168:171], a[108:111], a[8:11]\n\t"
      "ds_read_b128 v[228:231], v91 offset:9248\n\t"
      "v_mfma_f32_16x16x32_bf16 a[8:11], v[172:175], a[124:127], a[8:11]\n\t"
      "buffer_load_dwordx4 v131, s[68:71], s73 offen offset:3072 lds\n\t"
      "v_mfma_f32_16x16x32_bf16 a[8:11], v[176:179], a[128:131], a[8:11]\n\t"
      "ds_read_b128 v[232:235], v92 offset:9264\n\t"
      "v_mfma_f32_16x16x32_bf16 a[8:11], v[180:183], a[132:135], a[8:11]\n\t"
      "ds_read_b128 v[236:239], v89 offset:9280\n\t"
      "v_mfma_f32_16x16x32_bf16 a[16:19], v[148:151], a[88:91], a[16:19]\n\t"
      "v_mfma_f32_16x16x32_bf16 a[16:19], v[152:155], a[92:95], a[16:19]\n\t"
      "ds_read_b128 v[240:243], v90 offset:9296\n\t"
      "s_add_u32 m0, %[wb], 4096\n\t"
      "v_mfma_f32_16x16x32_bf16 a[16:19], v[156:159], a[96:99], a[16:19]\n\t"
      "buffer_load_dwordx4 v132, s[68:71], s73 offen offset:0 lds\n\t"
      "v_mfma_f32_16x16x32_bf16 a[16:19], v[160:163], a[112:115], a[16:19]\n\t"
      "ds_read_b128 v[244:247], v91 offset:9312\n\t"
      "v_mfma_f32_16x16x32_bf16 a[16:19], v[164:167], a[116:119], a[16:19]\n\t"
      "s_waitcnt lgkmcnt(4)\n\t"
      "ds_read_b128 v[248:251], v92 offset:9328\n\t"
      "v_mfma_f32_16x16x32_bf16 a[16:19], v[168:171], a[120:123], a[16:19]\n\t"
      "ds_read_b128 v[252:255], v89 offset:9344\n\t"
      "v_perm_b32 v184, v224, v220, s76\n\t"
      "v_mfma_f32_16x16x32_bf16 a[16:19], v[172:175], a[136:139], a[16:19]\n\t"
      "v_perm_b32 v188, v225, v220, s77\n\t"
      "v_perm_b32 v192, v225, v221, s76\n\t"
      "v_mfma_f32_16x16x32_bf16 a[16:19], v[176:179], a[140:143], a[16:19]\n\t"
      "s_waitcnt lgkmcnt(4)\n\t"
      "buffer_load_dwordx4 v133, s[68:71], s73 offen offset:1024 lds\n\t"
      "v_mfma_f32_16x16x32_bf16 a[16:19], v[180:183], a[144:147], a[16:19]\n\t"
      "s_waitcnt vmcnt(8)\n\t"
      "v_perm_b32 v196, v226, v221, s77\n\t"
      "v_mfma_f32_16x16x32_bf16 a[24:27], v[148:151], a[100:103], a[24:27]\n\t"
      "ds_read_b128 a[160:163], v143 offset:8448\n\t"
      "v_perm_b32 v200, v226, v222, s76\n\t"
      "v_mfma_f32_16x16x32_bf16 a[24:27], v[152:155], a[104:107], a[24:27]\n\t"
      "v_perm_b32 v204, v227, v222, s77\n\t"
      "v_perm_b32 v208, v227, v223, s76\n\t"
      "v_mfma_f32_16x16x32_bf16 a[24:27], v[156:159], a[108:111], a[24:27]\n\t"
      "s_waitcnt lgkmcnt(3)\n\t"
      "buffer_load_dwordx4 v134, s[68:71], s73 offen offset:2048 lds\n\t"
      "v_mfma_f32_16x16x32_bf16 a[24:27], v[160:163], a[124:127], a[24:27]\n\t"
      "ds_read_b128 a[164:167], v144 offset:8448\n\t"
      "v_perm_b32 v212, v228, v223, s77\n\t"
      "v_mfma_f32_16x16x32_bf16 a[24:27], v[164:167], a[128:131], a[24:27]\n\t"
      "s_waitcnt lgkmcnt(2)\n\t"
      "ds_read_b128 a[168:171], v145 offset:8448\n\t"
      "v_mfma_f32_16x16x32_bf16 a[24:27], v[168:171], a[132:135], a[24:27]\n\t"
      "v_perm_b32 v216, v228, v224, s76\n\t"
      "v_perm_b32 v185, v233, v229, s76\n\t"
      "v_mfma_f32_16x16x32_bf16 a[24:27], v[172:175], a[148:151], a[24:27]\n\t"
      "ds_read_b128 a[172:175], v143 offset:9472\n\t"
      "v_perm_b32 v189, v234, v229, s77\n\t"
      "v_mfma_f32_16x16x32_bf16 a[24:27], v[176:179], a[152:155], a[24:27]\n\t"
      "buffer_load_dwordx4 v135, s[68:71], s73 offen offset:3072 lds\n\t"
      "v_mfma_f32_16x16x32_bf16 a[24:27], v[180:183], a[156:159], a[24:27]\n\t"
      "ds_read_b128 a[176:179], v144 offset:9472\n\t"
      "v_perm_b32 v193, v234, v230, s76\n\t"
      "v_mfma_f32_16x16x32_bf16 a[32:35], v[148:151], a[112:115], a[32:35]\n\t"
      "v_perm_b32 v197, v235, v230, s77\n\t"
      "v_mfma_f32_16x16x32_bf16 a[32:35], v[152:155], a[116:119], a[32:35]\n\t"
      "ds_read_b128 a[180:183], v145 offset:9472\n\t"
      "v_perm_b32 v201, v235, v231, s76\n\t"
      "v_mfma_f32_16x16x32_bf16 a[32:35], v[156:159], a[120:123], a[32:35]\n\t"
      "v_perm_b32 v205, v236, v231, s77\n\t"
      "s_add_u32 m0, %[wb], 8192\n\t"
      "v_mfma_f32_16x16x32_bf16 a[32:35], v[160:163], a[136:139], a[32:35]\n\t"
      "buffer_load_dwordx4 v136, s[68:71], s73 offen offset:0 lds\n\t"
      "v_mfma_f32_16x16x32_bf16 a[32:35], v[164:167], a[140:143], a[32:35]\n\t"
      "s_waitcnt lgkmcnt(3)\n\t"
      "v_perm_b32 v209, v236, v232, s76\n\t"
      "v_mfma_f32_16x16x32_bf16 a[32:35], v[168:171], a[144:147], a[32:35]\n\t"
      "v_perm_b32 v213, v237, v232, s77\n\t"
      "v_mfma_f32_16x16x32_bf16 a[32:35], v[172:175], a[160:163], a[32:35]\n\t"
      "s_cbranch_vccnz 80f\n\t"
      "v_perm_b32 v217, v237, v233, s76\n\t"
      "v_mfma_f32_16x16x32_bf16 a[32:35], v[176:179], a[164:167], a[32:35]\n\t"
      "s_waitcnt vmcnt(9)\n\t"
      "s_add_u32 m0, %[wb], 18496\n\t"
      "v_mfma_f32_16x16x32_bf16 a[32:35], v[180:183], a[168:171], a[32:35]\n\t"
      "buffer_load_dwordx4 v137, %[asrd], s74 offen offset:0 sc1 lds\n\t"
      "v_mfma_f32_16x16x32_bf16 a[40:43], v[148:151], a[124:127], a[40:43]\n\t"
      "ds_read_b128 a[184:187], v143 offset:10560\n\t"
      "v_perm_b32 v186, v242, v238, s76\n\t"
      "v_mfma_f32_16x16x32_bf16 a[40:43], v[152:155], a[128:131], a[40:43]\n\t"
      "ds_read_b128 a[188:191], v144 offset:10560\n\t"
      "v_perm_b32 v190, v243, v238, s77\n\t"
      "v_mfma_f32_16x16x32_bf16 a[40:43], v[156:159], a[132:135], a[40:43]\n\t"
      "v_perm_b32 v194, v243, v239, s76\n\t"
      "v_mfma_f32_16x16x32_bf16 a[40:43], v[160:163], a[148:151], a[40:43]\n\t"
      "ds_read_b128 a[192:195], v145 offset:10560\n\t"
      "v_perm_b32 v198, v244, v239, s77\n\t"
      "v_mfma_f32_16x16x32_bf16 a[40:43], v[164:167], a[152:155], a[40:43]\n\t"
      "s_waitcnt lgkmcnt(3)\n\t"
      "buffer_load_dwordx4 v137, %[asrd], s74 offen offset:1024 sc1 lds\n\t"
      "v_mfma_f32_16x16x32_bf16 a[40:43], v[168:171], a[156:159], a[40:43]\n\t"
      "ds_read_b128 a[196:199], v143 offset:11584\n\t"
      "v_perm_b32 v202, v244, v240, s76\n\t"
      "v_mfma_f32_16x16x32_bf16 a[40:43], v[172:175], a[172:175], a[40:43]\n\t"
      "v_perm_b32 v206, v245, v240, s77\n\t"
      "v_mfma_f32_16x16x32_bf16 a[40:43], v[176:179], a[176:179], a[40:43]\n\t"
      "ds_read_b128 a[200:203], v144 offset:11584\n\t"
      "s_add_u32 m0, %[wb], 20608\n\t"
      "v_mfma_f32_16x16x32_bf16 a[40:43], v[180:183], a[180:183], a[40:43]\n\t"
      "buffer_load_dwordx4 v138, %[asrd], s74 offen offset:0 sc1 lds\n\t"
      "v_mfma_f32_16x16x32_bf16 a[48:51], v[148:151], a[136:139], a[48:51]\n\t"
      "ds_read_b128 a[204:207], v145 offset:11584\n\t"
      "v_perm_b32 v210, v245, v241, s76\n\t"
      "v_mfma_f32_16x16x32_bf16 a[48:51], v[152:155], a[140:143], a[48:51]\n\t"
      "v_perm_b32 v214, v246, v241, s77\n\t"
      "v_mfma_f32_16x16x32_bf16 a[48:51], v[156:159], a[144:147], a[48:51]\n\t"
      "v_perm_b32 v218, v246, v242, s76\n\t"
      "v_mfma_f32_16x16x32_bf16 a[48:51], v[160:163], a[160:163], a[48:51]\n\t"
      "v_perm_b32 v187, v251, v247, s76\n\t"
      "v_mfma_f32_16x16x32_bf16 a[48:51], v[164:167], a[164:167], a[48:51]\n\t"
      "s_waitcnt lgkmcnt(2)\n\t"
      "buffer_load_dwordx4 v138, %[asrd], s74 offen offset:1024 sc1 lds\n\t"
      "v_mfma_f32_16x16x32_bf16 a[48:51], v[168:171], a[168:171], a[48:51]\n\t"
      "v_perm_b32 v191, v252, v247, s77\n\t"
      "v_mfma_f32_16x16x32_bf16 a[48:51], v[172:175], a[184:187], a[48:51]\n\t"
      "v_perm_b32 v195, v252, v248, s76\n\t"
      "v_mfma_f32_16x16x32_bf16 a[48:51], v[176:179], a[188:191], a[48:51]\n\t"
      "v_perm_b32 v199, v253, v248, s77\n\t"
      "v_mfma_f32_16x16x32_bf16 a[48:51], v[180:183], a[192:195], a[48:51]\n\t"
      "v_perm_b32 v203, v253, v249, s76\n\t"
      "s_add_u32 m0, %[wb], 22720\n\t"
      "v_mfma_f32_16x16x32_bf16 a[56:59], v[148:151], a[148:151], a[56:59]\n\t"
      "buffer_load_dwordx4 v139, %[asrd], s74 offen offset:0 sc1 lds\n\t"
      "v_mfma_f32_16x16x32_bf16 a[56:59], v[152:155], a[152:155], a[56:59]\n\t"
      "v_perm_b32 v207, v254, v249, s77\n\t"
      "v_mfma_f32_16x16x32_bf16 a[56:59], v[156:159], a[156:159], a[56:59]\n\t"
      "v_perm_b32 v211, v254, v250, s76\n\t"
      "v_mfma_f32_16x16x32_bf16 a[56:59], v[160:163], a[172:175], a[56:59]\n\t"
      "v_perm_b32 v215, v255, v250, s77\n\t"
      "v_mfma_f32_16x16x32_bf16 a[56:59], v[164:167], a[176:179], a[56:59]\n\t"
      "s_waitcnt lgkmcnt(0)\n\t"
      "buffer_load_dwordx4 v139, %[asrd], s74 offen offset:1024 sc1 lds\n\t"
      "v_mfma_f32_16x16x32_bf16 a[56:59], v[168:171], a[180:183], a[56:59]\n\t"
      "v_perm_b32 v219, v255, v251, s76\n\t"
      "v_mfma_f32_16x16x32_bf16 a[56:59], v[172:175], a[196:199], a[56:59]\n\t"
      "v_mfma_f32_16x16x32_bf16 a[56:59], v[176:179], a[200:203], a[56:59]\n\t"
      "v_mfma_f32_16x16x32_bf16 a[56:59], v[180:183], a[204:207], a[56:59]\n\t"
      "s_add_u32 m0, %[wb], 24832\n\t"
      "v_mfma_f32_16x16x32_bf16 a[4:7], v[184:187], a[64:67], a[4:7]\n\t"
      "buffer_load_dwordx4 v140, %[asrd], s74 offen offset:0 sc1 lds\n\t"
      "v_mfma_f32_16x16x32_bf16 a[4:7], v[188:191], a[68:71], a[4:7]\n\t"
      "v_mfma_f32_16x16x32_bf16 a[4:7], v[192:195], a[72:75], a[4:7]\n\t"
      "v_mfma_f32_16x16x32_bf16 a[4:7], v[196:199], a[88:91], a[4:7]\n\t"
      "v_mfma_f32_16x16x32_bf16 a[4:7], v[200:203], a[92:95], a[4:7]\n\t"
      "s_waitcnt vmcnt(7)\n\t"
      "v_mfma_f32_16x16x32_bf16 a[4:7], v[204:207], a[96:99], a[4:7]\n\t"
      "buffer_load_dwordx4 v140, %[asrd], s74 offen offset:1024 sc1 lds\n\t"
      "v_mfma_f32_16x16x32_bf16 a[4:7], v[208:211], a[112:115], a[4:7]\n\t"
      "ds_read_b128 v[220:223], v89 offset:0\n\t"
      "v_mfma_f32_16x16x32_bf16 a[4:7], v[212:215], a[116:119], a[4:7]\n\t"
      "ds_read_b128 v[224:227], v90 offset:16\n\t"
      "s_mov_b32 s64, s74\n\t"
      "v_mfma_f32_16x16x32_bf16 a[4:7], v[216:219], a[120:123], a[4:7]\n\t"
      "ds_read_b128 v[228:231], v91 offset:32\n\t"
      "v_mfma_f32_16x16x32_bf16 a[12:15], v[184:187], a[76:79], a[12:15]\n\t"
      "s_add_u32 s75, s75, 1\n\t"
      "s_add_u32 m0, %[wb], 9216\n\t"
      "v_mfma_f32_16x16x32_bf16 a[12:15], v[188:191], a[80:83], a[12:15]\n\t"
      "buffer_load_dwordx4 v128, s[68:71], s72 offen offset:0 lds\n\t"
      "v_mfma_f32_16x16x32_bf16 a[12:15], v[192:195], a[84:87], a[12:15]\n\t"
      "ds_read_b128 v[232:235], v92 offset:48\n\t"
      "s_lshr_b32 s67, s67, 2\n\t"
      "v_mfma_f32_16x16x32_bf16 a[12:15], v[196:199], a[100:103], a[12:15]\n\t"
      "ds_read_b128 v[236:239], v89 offset:64\n\t"
      "s_and_b32 s78, s67, 3\n\t"
      "v_mfma_f32_16x16x32_bf16 a[12:15], v[200:203], a[104:107], a[12:15]\n\t"
      "ds_read_b128 v[240:243], v90 offset:80\n\t"
      "s_mul_i32 s73, s78, 576\n\t"
      "v_mfma_f32_16x16x32_bf16 a[12:15], v[204:207], a[108:111], a[12:15]\n\t"
      "s_lshl_b32 s74, s78, 17\n\t"
      "v_mfma_f32_16x16x32_bf16 a[12:15], v[208:211], a[124:127], a[12:15]\n\t"
      "buffer_load_dwordx4 v129, s[68:71], s72 offen offset:1024 lds\n\t"
      "v_mfma_f32_16x16x32_bf16 a[12:15], v[212:215], a[128:131], a[12:15]\n\t"
      "s_waitcnt lgkmcnt(3)\n\t"
      "ds_read_b128 v[244:247], v91 offset:96\n\t"
      "v_mfma_f32_16x16x32_bf16 a[12:15], v[216:219], a[132:135], a[12:15]\n\t"
      "ds_read_b128 v[248:251], v92 offset:112\n\t"
      "s_add_u32 s73, s73, %[wq]\n\t"
      "v_mfma_f32_16x16x32_bf16 a[20:23], v[184:187], a[88:91], a[20:23]\n\t"
      "ds_read_b128 v[252:255], v89 offset:128\n\t"
      "v_perm_b32 v148, v224, v220, s76\n\t"
      "v_mfma_f32_16x16x32_bf16 a[20:23], v[188:191], a[92:95], a[20:23]\n\t"
      "buffer_load_dwordx4 v130, s[68:71], s72 offen offset:2048 lds\n\t"
      "v_mfma_f32_16x16x32_bf16 a[20:23], v[192:195], a[96:99], a[20:23]\n\t"
      "s_waitcnt lgkmcnt(4)\n\t"
      "v_perm_b32 v152, v225, v220, s77\n\t"
      "v_mfma_f32_16x16x32_bf16 a[20:23], v[196:199], a[112:115], a[20:23]\n\t"
      "v_perm_b32 v156, v225, v221, s76\n\t"
      "v_perm_b32 v160, v226, v221, s77\n\t"
      "v_mfma_f32_16x16x32_bf16 a[20:23], v[200:203], a[116:119], a[20:23]\n\t"
      "v_perm_b32 v164, v226, v222, s76\n\t"
      "v_mfma_f32_16x16x32_bf16 a[20:23], v[204:207], a[120:123], a[20:23]\n\t"
      "v_perm_b32 v168, v227, v222, s77\n\t"
      "v_mfma_f32_16x16x32_bf16 a[20:23], v[208:211], a[136:139], a[20:23]\n\t"
      "s_waitcnt lgkmcnt(2)\n\t"
      "buffer_load_dwordx4 v131, s[68:71], s72 offen offset:3072 lds\n\t"
      "v_mfma_f32_16x16x32_bf16 a[20:23], v[212:215], a[140:143], a[20:23]\n\t"
      "v_perm_b32 v172, v227, v223, s76\n\t"
      "v_mfma_f32_16x16x32_bf16 a[20:23], v[216:219], a[144:147], a[20:23]\n\t"
      "s_waitcnt lgkmcnt(0)\n\t"
      "v_perm_b32 v176, v228, v223, s77\n\t"
      "v_mfma_f32_16x16x32_bf16 a[28:31], v[184:187], a[100:103], a[28:31]\n\t"
      "s_waitcnt vmcnt(8)\n\t"
      "v_perm_b32 v180, v228, v224, s76\n\t"
      "v_mfma_f32_16x16x32_bf16 a[28:31], v[188:191], a[104:107], a[28:31]\n\t"
      "ds_read_b128 a[64:67], v143 offset:0\n\t"
      "s_add_u32 m0, %[wb], 13312\n\t"
      "v_mfma_f32_16x16x32_bf16 a[28:31], v[192:195], a[108:111], a[28:31]\n\t"
      "buffer_load_dwordx4 v132, s[68:71], s72 offen offset:0 lds\n\t"
      "v_mfma_f32_16x16x32_bf16 a[28:31], v[196:199], a[124:127], a[28:31]\n\t"
      "ds_read_b128 a[68:71], v144 offset:0\n\t"
      "v_perm_b32 v149, v233, v229, s76\n\t"
      "v_mfma_f32_16x16x32_bf16 a[28:31], v[200:203], a[128:131], a[28:31]\n\t"
      "ds_read_b128 a[72:75], v145 offset:0\n\t"
      "v_perm_b32 v153, v234, v229, s77\n\t"
      "v_mfma_f32_16x16x32_bf16 a[28:31], v[204:207], a[132:135], a[28:31]\n\t"
      "ds_read_b128 a[76:79], v143 offset:1024\n\t"
      "v_perm_b32 v157, v234, v230, s76\n\t"
      "v_mfma_f32_16x16x32_bf16 a[28:31], v[208:211], a[148:151], a[28:31]\n\t"
      "buffer_load_dwordx4 v133, s[68:71], s72 offen offset:1024 lds\n\t"
      "v_mfma_f32_16x16x32_bf16 a[28:31], v[212:215], a[152:155], a[28:31]\n\t"
      "ds_read_b128 a[80:83], v144 offset:1024\n\t"
      "v_perm_b32 v161, v235, v230, s77\n\t"
      "v_mfma_f32_16x16x32_bf16 a[28:31], v[216:219], a[156:159], a[28:31]\n\t"
      "ds_read_b128 a[84:87], v145 offset:1024\n\t"
      "v_perm_b32 v165, v235, v231, s76\n\t"
      "v_mfma_f32_16x16x32_bf16 a[36:39], v[184:187], a[112:115], a[36:39]\n\t"
      "v_perm_b32 v169, v236, v231, s77\n\t"
      "v_mfma_f32_16x16x32_bf16 a[36:39], v[188:191], a[116:119], a[36:39]\n\t"
      "ds_read_b128 a[88:91], v143 offset:2112\n\t"
      "v_perm_b32 v173, v236, v232, s76\n\t"
      "v_mfma_f32_16x16x32_bf16 a[36:39], v[192:195], a[120:123], a[36:39]\n\t"
      "buffer_load_dwordx4 v134, s[68:71], s72 offen offset:2048 lds\n\t"
      "v_mfma_f32_16x16x32_bf16 a[36:39], v[196:199], a[136:139], a[36:39]\n\t"
      "ds_read_b128 a[92:95], v144 offset:2112\n\t"
      "v_perm_b32 v177, v237, v232, s77\n\t"
      "v_mfma_f32_16x16x32_bf16 a[36:39], v[200:203], a[140:143], a[36:39]\n\t"
      "ds_read_b128 a[96:99], v145 offset:2112\n\t"
      "v_perm_b32 v181, v237, v233, s76\n\t"
      "v_mfma_f32_16x16x32_bf16 a[36:39], v[204:207], a[144:147], a[36:39]\n\t"
      "ds_read_b128 a[100:103], v143 offset:3136\n\t"
      "v_perm_b32 v150, v242, v238, s76\n\t"
      "v_mfma_f32_16x16x32_bf16 a[36:39], v[208:211], a[160:163], a[36:39]\n\t"
      "v_perm_b32 v154, v243, v238, s77\n\t"
      "v_mfma_f32_16x16x32_bf16 a[36:39], v[212:215], a[164:167], a[36:39]\n\t"
      "buffer_load_dwordx4 v135, s[68:71], s72 offen offset:3072 lds\n\t"
      "v_mfma_f32_16x16x32_bf16 a[36:39], v[216:219], a[168:171], a[36:39]\n\t"
      "ds_read_b128 a[104:107], v144 offset:3136\n\t"
      "v_perm_b32 v158, v243, v239, s76\n\t"
      "v_mfma_f32_16x16x32_bf16 a[44:47], v[184:187], a[124:127], a[44:47]\n\t"
      "ds_read_b128 a[108:111], v145 offset:3136\n\t"
      "v_perm_b32 v162, v244, v239, s77\n\t"
      "v_mfma_f32_16x16x32_bf16 a[44:47], v[188:191], a[128:131], a[44:47]\n\t"
      "v_perm_b32 v166, v244, v240, s76\n\t"
      "v_mfma_f32_16x16x32_bf16 a[44:47], v[192:195], a[132:135], a[44:47]\n\t"
      "v_perm_b32 v170, v245, v240, s77\n\t"
      "s_add_u32 m0, %[wb], 17408\n\t"
      "v_mfma_f32_16x16x32_bf16 a[44:47], v[196:199], a[148:151], a[44:47]\n\t"
      "s_waitcnt vmcnt(10)\n\t"
      "buffer_load_dwordx4 v136, s[68:71], s72 offen offset:0 lds\n\t"
      "v_mfma_f32_16x16x32_bf16 a[44:47], v[200:203], a[152:155], a[44:47]\n\t"
      "ds_read_b128 a[112:115], v143 offset:4224\n\t"
      "v_perm_b32 v174, v245, v241, s76\n\t"
      "v_mfma_f32_16x16x32_bf16 a[44:47], v[204:207], a[156:159], a[44:47]\n\t"
      "ds_read_b128 a[116:119], v144 offset:4224\n\t"
      "v_perm_b32 v178, v246, v241, s77\n\t"
      "v_mfma_f32_16x16x32_bf16 a[44:47], v[208:211], a[172:175], a[44:47]\n\t"
      "v_perm_b32 v182, v246, v242, s76\n\t"
      "v_mfma_f32_16x16x32_bf16 a[44:47], v[212:215], a[176:179], a[44:47]\n\t"
      "ds_read_b128 a[120:123], v145 offset:4224\n\t"
      "s_add_u32 m0, %[wb], 26944\n\t"
      "v_mfma_f32_16x16x32_bf16 a[44:47], v[216:219], a[180:183], a[44:47]\n\t"
      "s_add_u32 s72, s73, 147456\n\t"
      "buffer_load_dwordx4 v141, %[asrd], s64 offen offset:0 sc1 lds\n\t"
      "v_mfma_f32_16x16x32_bf16 a[52:55], v[184:187], a[136:139], a[52:55]\n\t"
      "ds_read_b128 a[124:127], v143 offset:5248\n\t"
      "s_cmp_eq_u32 s75, 3\n\t"
      "v_mfma_f32_16x16x32_bf16 a[52:55], v[188:191], a[140:143], a[52:55]\n\t"
      "ds_read_b128 a[128:131], v144 offset:5248\n\t"
      "s_cselect_b64 s[68:69], %[wn01], s[68:69]\n\t"
      "v_mfma_f32_16x16x32_bf16 a[52:55], v[192:195], a[144:147], a[52:55]\n\t"
      "ds_read_b128 a[132:135], v145 offset:5248\n\t"
      "s_cselect_b32 s70, %[wn2], s70\n\t"
      "v_mfma_f32_16x16x32_bf16 a[52:55], v[196:199], a[160:163], a[52:55]\n\t"
      "s_cselect_b64 vcc, -1, 0\n\t"
      "buffer_load_dwordx4 v141, %[asrd], s64 offen offset:1024 sc1 lds\n\t"
      "v_mfma_f32_16x16x32_bf16 a[52:55], v[200:203], a[164:167], a[52:55]\n\t"
      "v_perm_b32 v151, v251, v247, s76\n\t"
      "v_mfma_f32_16x16x32_bf16 a[52:55], v[204:207], a[168:171], a[52:55]\n\t"
      "v_perm_b32 v155, v252, v247, s77\n\t"
      "v_mfma_f32_16x16x32_bf16 a[52:55], v[208:211], a[184:187], a[52:55]\n\t"
      "v_perm_b32 v159, v252, v248, s76\n\t"
      "v_mfma_f32_16x16x32_bf16 a[52:55], v[212:215], a[188:191], a[52:55]\n\t"
      "v_perm_b32 v163, v253, v248, s77\n\t"
      "s_add_u32 m0, %[wb], 29056\n\t"
      "v_mfma_f32_16x16x32_bf16 a[52:55], v[216:219], a[192:195], a[52:55]\n\t"
      "buffer_load_dwordx4 v142, %[asrd], s64 offen offset:0 sc1 lds\n\t"
      "v_mfma_f32_16x16x32_bf16 a[60:63], v[184:187], a[148:151], a[60:63]\n\t"
      "v_perm_b32 v167, v253, v249, s76\n\t"
      "v_mfma_f32_16x16x32_bf16 a[60:63], v[188:191], a[152:155], a[60:63]\n\t"
      "v_perm_b32 v171, v254, v249, s77\n\t"
      "v_mfma_f32_16x16x32_bf16 a[60:63], v[192:195], a[156:159], a[60:63]\n\t"
      "v_perm_b32 v175, v254, v250, s76\n\t"
      "v_mfma_f32_16x16x32_bf16 a[60:63], v[196:199], a[172:175], a[60:63]\n\t"
      "v_perm_b32 v179, v255, v250, s77\n\t"
      "v_mfma_f32_16x16x32_bf16 a[60:63], v[200:203], a[176:179], a[60:63]\n\t"
      "buffer_load_dwordx4 v142, %[asrd], s64 offen offset:1024 sc1 lds\n\t"
      "v_mfma_f32_16x16x32_bf16 a[60:63], v[204:207], a[180:183], a[60:63]\n\t"
      "v_perm_b32 v183, v255, v251, s76\n\t"
      "v_mfma_f32_16x16x32_bf16 a[60:63], v[208:211], a[196:199], a[60:63]\n\t"
      "v_mfma_f32_16x16x32_bf16 a[60:63], v[212:215], a[200:203], a[60:63]\n\t"
      "s_waitcnt lgkmcnt(0)\n\t"
      "v_mfma_f32_16x16x32_bf16 a[60:63], v[216:219], a[204:207], a[60:63]\n\t"
      "s_branch 1b\n\t"
      "80:\n\t"
      "v_perm_b32 v217, v237, v233, s76\n\t"
      "v_mfma_f32_16x16x32_bf16 a[32:35], v[176:179], a[164:167], a[32:35]\n\t"
      "s_waitcnt vmcnt(9)\n\t"
      "s_add_u32 m0, %[wb], 9216\n\t"
      "v_mfma_f32_16x16x32_bf16 a[32:35], v[180:183], a[168:171], a[32:35]\n\t"
      "buffer_load_dwordx4 v128, s[68:71], s72 offen offset:0 lds\n\t"
      "v_mfma_f32_16x16x32_bf16 a[40:43], v[148:151], a[124:127], a[40:43]\n\t"
      "ds_read_b128 a[184:187], v143 offset:10560\n\t"
      "v_perm_b32 v186, v242, v238, s76\n\t"
      "v_mfma_f32_16x16x32_bf16 a[40:43], v[152:155], a[128:131], a[40:43]\n\t"
      "ds_read_b128 a[188:191], v144 offset:10560\n\t"
      "v_perm_b32 v190, v243, v238, s77\n\t"
      "v_mfma_f32_16x16x32_bf16 a[40:43], v[156:159], a[132:135], a[40:43]\n\t"
      "v_perm_b32 v194, v243, v239, s76\n\t"
      "v_mfma_f32_16x16x32_bf16 a[40:43], v[160:163], a[148:151], a[40:43]\n\t"
      "ds_read_b128 a[192:195], v145 offset:10560\n\t"
      "v_perm_b32 v198, v244, v239, s77\n\t"
      "v_mfma_f32_16x16x32_bf16 a[40:43], v[164:167], a[152:155], a[40:43]\n\t"
      "s_waitcnt lgkmcnt(3)\n\t"
      "buffer_load_dwordx4 v129, s[68:71], s72 offen offset:1024 lds\n\t"
      "v_mfma_f32_16x16x32_bf16 a[40:43], v[168:171], a[156:159], a[40:43]\n\t"
      "ds_read_b128 a[196:199], v143 offset:11584\n\t"
      "v_perm_b32 v202, v244, v240, s76\n\t"
      "v_mfma_f32_16x16x32_bf16 a[40:43], v[172:175], a[172:175], a[40:43]\n\t"
      "v_perm_b32 v206, v245, v240, s77\n\t"
      "v_mfma_f32_16x16x32_bf16 a[40:43], v[176:179], a[176:179], a[40:43]\n\t"
      "ds_read_b128 a[200:203], v144 offset:11584\n\t"
      "v_mfma_f32_16x16x32_bf16 a[40:43], v[180:183], a[180:183], a[40:43]\n\t"
      "buffer_load_dwordx4 v130, s[68:71], s72 offen offset:2048 lds\n\t"
      "v_mfma_f32_16x16x32_bf16 a[48:51], v[148:151], a[136:139], a[48:51]\n\t"
      "ds_read_b128 a[204:207], v145 offset:11584\n\t"
      "v_perm_b32 v210, v245, v241, s76\n\t"
      "v_mfma_f32_16x16x32_bf16 a[48:51], v[152:155], a[140:143], a[48:51]\n\t"
      "v_perm_b32 v214, v246, v241, s77\n\t"
      "v_mfma_f32_16x16x32_bf16 a[48:51], v[156:159], a[144:147], a[48:51]\n\t"
      "v_perm_b32 v218, v246, v242, s76\n\t"
      "v_mfma_f32_16x16x32_bf16 a[48:51], v[160:163], a[160:163], a[48:51]\n\t"
      "v_perm_b32 v187, v251, v247, s76\n\t"
      "v_mfma_f32_16x16x32_bf16 a[48:51], v[164:167], a[164:167], a[48:51]\n\t"
      "s_waitcnt lgkmcnt(2)\n\t"
      "buffer_load_dwordx4 v131, s[68:71], s72 offen offset:3072 lds\n\t"
      "v_mfma_f32_16x16x32_bf16 a[48:51], v[168:171], a[168:171], a[48:51]\n\t"
      "v_perm_b32 v191, v252, v247, s77\n\t"
      "v_mfma_f32_16x16x32_bf16 a[48:51], v[172:175], a[184:187], a[48:51]\n\t"
      "v_perm_b32 v195, v252, v248, s76\n\t"
      "v_mfma_f32_16x16x32_bf16 a[48:51], v[176:179], a[188:191], a[48:51]\n\t"
      "v_perm_b32 v199, v253, v248, s77\n\t"
      "v_mfma_f32_16x16x32_bf16 a[48:51], v[180:183], a[192:195], a[48:51]\n\t"
      "v_perm_b32 v203, v253, v249, s76\n\t"
      "s_add_u32 m0, %[wb], 13312\n\t"
      "v_mfma_f32_16x16x32_bf16 a[56:59], v[148:151], a[148:151], a[56:59]\n\t"
      "buffer_load_dwordx4 v132, s[68:71], s72 offen offset:0 lds\n\t"
      "v_mfma_f32_16x16x32_bf16 a[56:59], v[152:155], a[152:155], a[56:59]\n\t"
      "v_perm_b32 v207, v254, v249, s77\n\t"
      "v_mfma_f32_16x16x32_bf16 a[56:59], v[156:159], a[156:159], a[56:59]\n\t"
      "v_perm_b32 v211, v254, v250, s76\n\t"
      "v_mfma_f32_16x16x32_bf16 a[56:59], v[160:163], a[172:175], a[56:59]\n\t"
      "v_perm_b32 v215, v255, v250, s77\n\t"
      "v_mfma_f32_16x16x32_bf16 a[56:59], v[164:167], a[176:179], a[56:59]\n\t"
      "s_waitcnt lgkmcnt(0)\n\t"
      "buffer_load_dwordx4 v133, s[68:71], s72 offen offset:1024 lds\n\t"
      "v_mfma_f32_16x16x32_bf16 a[56:59], v[168:171], a[180:183], a[56:59]\n\t"
      "v_perm_b32 v219, v255, v251, s76\n\t"
      "v_mfma_f32_16x16x32_bf16 a[56:59], v[172:175], a[196:199], a[56:59]\n\t"
      "v_mfma_f32_16x16x32_bf16 a[56:59], v[176:179], a[200:203], a[56:59]\n\t"
      "v_mfma_f32_16x16x32_bf16 a[56:59], v[180:183], a[204:207], a[56:59]\n\t"
      "v_mfma_f32_16x16x32_bf16 a[4:7], v[184:187], a[64:67], a[4:7]\n\t"
      "buffer_load_dwordx4 v134, s[68:71], s72 offen offset:2048 lds\n\t"
      "v_mfma_f32_16x16x32_bf16 a[4:7], v[188:191], a[68:71], a[4:7]\n\t"
      "v_mfma_f32_16x16x32_bf16 a[4:7], v[192:195], a[72:75], a[4:7]\n\t"
      "v_mfma_f32_16x16x32_bf16 a[4:7], v[196:199], a[88:91], a[4:7]\n\t"
      "v_mfma_f32_16x16x32_bf16 a[4:7], v[200:203], a[92:95], a[4:7]\n\t"
      "v_mfma_f32_16x16x32_bf16 a[4:7], v[204:207], a[96:99], a[4:7]\n\t"
      "buffer_load_dwordx4 v135, s[68:71], s72 offen offset:3072 lds\n\t"
      "v_mfma_f32_16x16x32_bf16 a[4:7], v[208:211], a[112:115], a[4:7]\n\t"
      "v_mfma_f32_16x16x32_bf16 a[4:7], v[212:215], a[116:119], a[4:7]\n\t"
      "v_mfma_f32_16x16x32_bf16 a[4:7], v[216:219], a[120:123], a[4:7]\n\t"
      "v_mfma_f32_16x16x32_bf16 a[12:15], v[184:187], a[76:79], a[12:15]\n\t"
      "s_add_u32 m0, %[wb], 17408\n\t"
      "v_mfma_f32_16x16x32_bf16 a[12:15], v[188:191], a[80:83], a[12:15]\n\t"
      "buffer_load_dwordx4 v136, s[68:71], s72 offen offset:0 lds\n\t"
      "v_mfma_f32_16x16x32_bf16 a[12:15], v[192:195], a[84:87], a[12:15]\n\t"
      "v_mfma_f32_16x16x32_bf16 a[12:15], v[196:199], a[100:103], a[12:15]\n\t"
      "v_mfma_f32_16x16x32_bf16 a[12:15], v[200:203], a[104:107], a[12:15]\n\t"
      "v_mfma_f32_16x16x32_bf16 a[12:15], v[204:207], a[108:111], a[12:15]\n\t"
      "v_mfma_f32_16x16x32_bf16 a[12:15], v[208:211], a[124:127], a[12:15]\n\t"
      "v_mfma_f32_16x16x32_bf16 a[12:15], v[212:215], a[128:131], a[12:15]\n\t"
      "v_mfma_f32_16x16x32_bf16 a[12:15], v[216:219], a[132:135], a[12:15]\n\t"
      "v_mfma_f32_16x16x32_bf16 a[20:23], v[184:187], a[88:91], a[20:23]\n\t"
      "v_mfma_f32_16x16x32_bf16 a[20:23], v[188:191], a[92:95], a[20:23]\n\t"
      "ds_write_b128 v147, a[0:3] offset:0\n\t"
      "v_mfma_f32_16x16x32_bf16 a[20:23], v[192:195], a[96:99], a[20:23]\n\t"
      "v_mfma_f32_16x16x32_bf16 a[20:23], v[196:199], a[112:115], a[20:23]\n\t"
      "v_mfma_f32_16x16x32_bf16 a[20:23], v[200:203], a[116:119], a[20:23]\n\t"
      "ds_write_b128 v147, a[8:11] offset:1024\n\t"
      "v_mfma_f32_16x16x32_bf16 a[20:23], v[204:207], a[120:123], a[20:23]\n\t"
      "v_mfma_f32_16x16x32_bf16 a[20:23], v[208:211], a[136:139], a[20:23]\n\t"
      "v_mfma_f32_16x16x32_bf16 a[20:23], v[212:215], a[140:143], a[20:23]\n\t"
      "ds_write_b128 v147, a[16:19] offset:2048\n\t"
      "v_mfma_f32_16x16x32_bf16 a[20:23], v[216:219], a[144:147], a[20:23]\n\t"
      "v_mfma_f32_16x16x32_bf16 a[28:31], v[184:187], a[100:103], a[28:31]\n\t"
      "v_mfma_f32_16x16x32_bf16 a[28:31], v[188:191], a[104:107], a[28:31]\n\t"
      "ds_write_b128 v147, a[24:27] offset:3072\n\t"
      "v_mfma_f32_16x16x32_bf16 a[28:31], v[192:195], a[108:111], a[28:31]\n\t"
      "v_mfma_f32_16x16x32_bf16 a[28:31], v[196:199], a[124:127], a[28:31]\n\t"
      "v_mfma_f32_16x16x32_bf16 a[28:31], v[200:203], a[128:131], a[28:31]\n\t"
      "ds_write_b128 v147, a[32:35] offset:4096\n\t"
      "v_mfma_f32_16x16x32_bf16 a[28:31], v[204:207], a[132:135], a[28:31]\n\t"
      "v_mfma_f32_16x16x32_bf16 a[28:31], v[208:211], a[148:151], a[28:31]\n\t"
      "v_mfma_f32_16x16x32_bf16 a[28:31], v[212:215], a[152:155], a[28:31]\n\t"
      "ds_write_b128 v147, a[40:43] offset:5120\n\t"
      "v_mfma_f32_16x16x32_bf16 a[28:31], v[216:219], a[156:159], a[28:31]\n\t"
      "v_mfma_f32_16x16x32_bf16 a[36:39], v[184:187], a[112:115], a[36:39]\n\t"
      "v_mfma_f32_16x16x32_bf16 a[36:39], v[188:191], a[116:119], a[36:39]\n\t"
      "ds_write_b128 v147, a[48:51] offset:6144\n\t"
      "v_mfma_f32_16x16x32_bf16 a[36:39], v[192:195], a[120:123], a[36:39]\n\t"
      "v_mfma_f32_16x16x32_bf16 a[36:39], v[196:199], a[136:139], a[36:39]\n\t"
      "v_mfma_f32_16x16x32_bf16 a[36:39], v[200:203], a[140:143], a[36:39]\n\t"
      "ds_write_b128 v147, a[56:59] offset:7168\n\t"
      "v_mfma_f32_16x16x32_bf16 a[36:39], v[204:207], a[144:147], a[36:39]\n\t"
      "v_mfma_f32_16x16x32_bf16 a[36:39], v[208:211], a[160:163], a[36:39]\n\t"
      "v_mfma_f32_16x16x32_bf16 a[36:39], v[212:215], a[164:167], a[36:39]\n\t"
      "ds_write_b128 v147, a[4:7] offset:8192\n\t"
      "v_mfma_f32_16x16x32_bf16 a[36:39], v[216:219], a[168:171], a[36:39]\n\t"
      "v_mfma_f32_16x16x32_bf16 a[44:47], v[184:187], a[124:127], a[44:47]\n\t"
      "v_mfma_f32_16x16x32_bf16 a[44:47], v[188:191], a[128:131], a[44:47]\n\t"
      "ds_write_b128 v147, a[12:15] offset:9216\n\t"
      "v_mfma_f32_16x16x32_bf16 a[44:47], v[192:195], a[132:135], a[44:47]\n\t"
      "v_mfma_f32_16x16x32_bf16 a[44:47], v[196:199], a[148:151], a[44:47]\n\t"
      "v_mfma_f32_16x16x32_bf16 a[44:47], v[200:203], a[152:155], a[44:47]\n\t"
      "ds_write_b128 v147, a[20:23] offset:10240\n\t"
      "v_mfma_f32_16x16x32_bf16 a[44:47], v[204:207], a[156:159], a[44:47]\n\t"
      "v_mfma_f32_16x16x32_bf16 a[44:47], v[208:211], a[172:175], a[44:47]\n\t"
      "v_mfma_f32_16x16x32_bf16 a[44:47], v[212:215], a[176:179], a[44:47]\n\t"
      "ds_write_b128 v147, a[28:31] offset:11264\n\t"
      "v_mfma_f32_16x16x32_bf16 a[44:47], v[216:219], a[180:183], a[44:47]\n\t"
      "v_mfma_f32_16x16x32_bf16 a[52:55], v[184:187], a[136:139], a[52:55]\n\t"
      "v_mfma_f32_16x16x32_bf16 a[52:55], v[188:191], a[140:143], a[52:55]\n\t"
      "ds_write_b128 v147, a[36:39] offset:12288\n\t"
      "v_mfma_f32_16x16x32_bf16 a[52:55], v[192:195], a[144:147], a[52:55]\n\t"
      "v_mfma_f32_16x16x32_bf16 a[52:55], v[196:199], a[160:163], a[52:55]\n\t"
      "v_mfma_f32_16x16x32_bf16 a[52:55], v[200:203], a[164:167], a[52:55]\n\t"
      "v_mfma_f32_16x16x32_bf16 a[52:55], v[204:207], a[168:171], a[52:55]\n\t"
      "v_mfma_f32_16x16x32_bf16 a[52:55], v[208:211], a[184:187], a[52:55]\n\t"
      "v_mfma_f32_16x16x32_bf16 a[52:55], v[212:215], a[188:191], a[52:55]\n\t"
      "ds_write_b128 v147, a[44:47] offset:13312\n\t"
      "v_mfma_f32_16x16x32_bf16 a[52:55], v[216:219], a[192:195], a[52:55]\n\t"
      "v_mfma_f32_16x16x32_bf16 a[60:63], v[184:187], a[148:151], a[60:63]\n\t"
      "v_mfma_f32_16x16x32_bf16 a[60:63], v[188:191], a[152:155], a[60:63]\n\t"
      "v_mfma_f32_16x16x32_bf16 a[60:63], v[192:195], a[156:159], a[60:63]\n\t"
      "v_mfma_f32_16x16x32_bf16 a[60:63], v[196:199], a[172:175], a[60:63]\n\t"
      "v_mfma_f32_16x16x32_bf16 a[60:63], v[200:203], a[176:179], a[60:63]\n\t"
      "v_mfma_f32_16x16x32_bf16 a[60:63], v[204:207], a[180:183], a[60:63]\n\t"
      "v_mfma_f32_16x16x32_bf16 a[60:63], v[208:211], a[196:199], a[60:63]\n\t"
      "v_mfma_f32_16x16x32_bf16 a[60:63], v[212:215], a[200:203], a[60:63]\n\t"
      "ds_write_b128 v147, a[52:55] offset:14336\n\t"
      "v_mfma_f32_16x16x32_bf16 a[60:63], v[216:219], a[204:207], a[60:63]\n\t"
      "s_nop 7\n\t"
      "ds_write_b128 v147, a[60:63] offset:15360\n\t"
      "s_waitcnt lgkmcnt(0)\n\t"
      "s_barrier\n\t"
      "ds_read_b128 v[148:151], v125 offset:0\n\t"
      "ds_read_b128 v[152:155], v125 offset:40960\n\t"
      "ds_read_b128 v[156:159], v126 offset:0\n\t"
      "ds_read_b128 v[160:163], v126 offset:40960\n\t"
      "ds_read_b128 v[164:167], v125 offset:1024\n\t"
      "ds_read_b128 v[168:171], v125 offset:41984\n\t"
      "ds_read_b128 v[172:175], v126 offset:1024\n\t"
      "ds_read_b128 v[176:179], v126 offset:41984\n\t"
      "ds_read_b128 v[220:223], v125 offset:8192\n\t"
      "ds_read_b128 v[224:227], v125 offset:49152\n\t"
      "ds_read_b128 v[228:231], v126 offset:8192\n\t"
      "ds_read_b128 v[232:235], v126 offset:49152\n\t"
      "ds_read_b128 v[236:239], v125 offset:9216\n\t"
      "ds_read_b128 v[240:243], v125 offset:50176\n\t"
      "ds_read_b128 v[244:247], v126 offset:9216\n\t"
      "ds_read_b128 v[248:251], v126 offset:50176\n\t"
      "s_waitcnt lgkmcnt(12)\n\t"
      "v_pk_add_f32 v[148:149], v[148:149], v[152:153]\n\t"
      "v_pk_add_f32 v[150:151], v[150:151], v[154:155]\n\t"
      "v_pk_add_f32 v[156:157], v[156:157], v[160:161]\n\t"
      "v_pk_add_f32 v[158:159], v[158:159], v[162:163]\n\t"
      "v_pk_add_f32 v[96:97], v[148:149], v[156:157]\n\t"
      "v_pk_add_f32 v[98:99], v[150:151], v[158:159]\n\t"
      "s_waitcnt lgkmcnt(8)\n\t"
      "v_pk_add_f32 v[164:165], v[164:165], v[168:169]\n\t"
      "v_pk_add_f32 v[166:167], v[166:167], v[170:171]\n\t"
      "v_pk_add_f32 v[172:173], v[172:173], v[176:177]\n\t"
      "v_pk_add_f32 v[174:175], v[174:175], v[178:179]\n\t"
      "v_pk_add_f32 v[104:105], v[164:165], v[172:173]\n\t"
      "v_pk_add_f32 v[106:107], v[166:167], v[174:175]\n\t"
      "s_waitcnt lgkmcnt(4)\n\t"
      "v_pk_add_f32 v[220:221], v[220:221], v[224:225]\n\t"
      "v_pk_add_f32 v[222:223], v[222:223], v[226:227]\n\t"
      "v_pk_add_f32 v[228:229], v[228:229], v[232:233]\n\t"
      "v_pk_add_f32 v[230:231], v[230:231], v[234:235]\n\t"
      "v_pk_add_f32 v[100:101], v[220:221], v[228:229]\n\t"
      "v_pk_add_f32 v[102:103], v[222:223], v[230:231]\n\t"
      "s_waitcnt lgkmcnt(0)\n\t"
      "v_pk_add_f32 v[236:237], v[236:237], v[240:241]\n\t"
      "v_pk_add_f32 v[238:239], v[238:239], v[242:243]\n\t"
      "v_pk_add_f32 v[244:245], v[244:245], v[248:249]\n\t"
      "v_pk_add_f32 v[246:247], v[246:247], v[250:251]\n\t"
      "v_pk_add_f32 v[108:109], v[236:237], v[244:245]\n\t"
      "v_pk_add_f32 v[110:111], v[238:239], v[246:247]\n\t"
          :: [wb] "s"(wb), [asrd] "s"(asrd), [wc01] "s"(wc), [wn01] "s"(wn), [wn2] "s"(wn2), [wq] "s"(wq), [a0] "s"(((order >> (8 * k)) & 3) << 17), [seq] "s"(order >> (8 * k + 2))
          : "memory", "scc", "vcc", "v255", "s64", "s65", "s66", "s67", "s68", "s69", "s70", "s71", "s72", "s73", "s74", "s75", "s76", "s77", "s78", "a0", "a1", "a2", "a3", "a4", "a5", "a6", "a7", "a8", "a9", "a10", "a11", "a12", "a13", "a14", "a15", "a16", "a17", "a18", "a19", "a20", "a21", "a22", "a23", "a24", "a25", "a26", "a27", "a28", "a29", "a30", "a31", "a32", "a33", "a34", "a35", "a36", "a37", "a38", "a39", "a40", "a41", "a42", "a43", "a44", "a45", "a46", "a47", "a48", "a49", "a50", "a51", "a52", "a53", "a54", "a55", "a56", "a57", "a58", "a59", "a60", "a61", "a62", "a63", "a64", "a65", "a66", "a67", "a68", "a69", "a70", "a71", "a72", "a73", "a74", "a75", "a76", "a77", "a78", "a79", "a80", "a81", "a82", "a83", "a84", "a85", "a86", "a87", "a88", "a89", "a90", "a91", "a92", "a93", "a94", "a95", "a96", "a97", "a98", "a99", "a100", "a101", "a102", "a103", "a104", "a105", "a106", "a107", "a108", "a109", "a110", "a111", "a112", "a113", "a114", "a115", "a116", "a117", "a118", "a119", "a120", "a121", "a122", "a123", "a124", "a125", "a126", "a127", "a128", "a129", "a130", "a131", "a132", "a133", "a134", "a135", "a136", "a137", "a138", "a139", "a140", "a141", "a142", "a143", "a144", "a145", "a146", "a147", "a148", "a149", "a150", "a151", "a152", "a153", "a154", "a155", "a156", "a157", "a158", "a159", "a160", "a161", "a162", "a163", "a164", "a165", "a166", "a167", "a168", "a169", "a170", "a171", "a172", "a173", "a174", "a175", "a176", "a177", "a178", "a179", "a180", "a181", "a182", "a183", "a184", "a185", "a186", "a187", "a188", "a189", "a190", "a191", "a192", "a193", "a194", "a195", "a196", "a197", "a198", "a199", "a200", "a201", "a202", "a203", "a204", "a205", "a206", "a207");
    }
  }
  asm volatile(
      "v_pk_add_f32 v[96:97], v[96:97], v[48:49]\n\t"
      "v_pk_add_f32 v[98:99], v[98:99], v[50:51]\n\t"
      "v_pk_add_f32 v[100:101], v[100:101], v[52:53]\n\t"
      "v_pk_add_f32 v[102:103], v[102:103], v[54:55]\n\t"
      "v_pk_add_f32 v[104:105], v[104:105], v[56:57]\n\t"
      "v_pk_add_f32 v[106:107], v[106:107], v[58:59]\n\t"
      "v_pk_add_f32 v[108:109], v[108:109], v[60:61]\n\t"
      "v_pk_add_f32 v[110:111], v[110:111], v[62:63]\n\t"
      "v_cvt_pk_bf16_f32 v184, v96, v97\n\t"
      "v_cvt_pk_bf16_f32 v185, v98, v99\n\t"
      "v_cvt_pk_bf16_f32 v186, v100, v101\n\t"
      "v_cvt_pk_bf16_f32 v187, v102, v103\n\t"
      "v_cvt_pk_bf16_f32 v188, v104, v105\n\t"
      "v_cvt_pk_bf16_f32 v189, v106, v107\n\t"
      "v_cvt_pk_bf16_f32 v190, v108, v109\n\t"
      "v_cvt_pk_bf16_f32 v191, v110, v111\n\t"
      "ds_write_b16 v72, v184 offset:0\n\t"
      "ds_write_b16_d16_hi v72, v184 offset:272\n\t"
      "ds_write_b16 v72, v185 offset:544\n\t"
      "ds_write_b16_d16_hi v72, v185 offset:816\n\t"
      "ds_write_b16 v73, v186 offset:0\n\t"
      "ds_write_b16_d16_hi v73, v186 offset:272\n\t"
      "ds_write_b16 v73, v187 offset:544\n\t"
      "ds_write_b16_d16_hi v73, v187 offset:816\n\t"
      "ds_write_b16 v72, v188 offset:32\n\t"
      "ds_write_b16_d16_hi v72, v188 offset:304\n\t"
      "ds_write_b16 v72, v189 offset:576\n\t"
      "ds_write_b16_d16_hi v72, v189 offset:848\n\t"
      "ds_write_b16 v73, v190 offset:32\n\t"
      "ds_write_b16_d16_hi v73, v190 offset:304\n\t"
      "ds_write_b16 v73, v191 offset:576\n\t"
      "ds_write_b16_d16_hi v73, v191 offset:848\n\t"
      "v_add_u32 v72, 0x2000, v75\n\t"
      "s_waitcnt lgkmcnt(0)\n\t"
      "s_barrier\n\t"
      "ds_read_b128 v[192:195], v74\n\t"
      "ds_read_b128 v[196:199], v74 offset:1088\n\t"
      "s_waitcnt lgkmcnt(1)\n\t"
      "global_store_dwordx4 v75, v[192:195], %[ob] sc1\n\t"
      "s_waitcnt lgkmcnt(0)\n\t"
      "global_store_dwordx4 v72, v[196:199], %[ob] sc1\n\t"
      ".Lpf_end:"
      ::[ob] "s"(out + (size_t)(b * 512 + o0) * 2048 + mt * 256) : "memory");
}

extern "C" void kernel_launch(void* const* d_in, const int* in_sizes, int n_in, void* d_out, int out_size,
                              void* d_ws, size_t ws_size, hipStream_t stream) {
  (void)hipFuncSetAttribute((const void*)k_resblock, hipFuncAttributeMaxDynamicSharedMemorySize, 4 * WREG);
  k_resblock<<<256, 256, 4 * WREG, stream>>>((const char*)d_in[0], (const char*)d_in[1], (const char*)d_in[2],
                                             (const char*)d_in[3], (const char*)d_in[4], (const char*)d_in[5],
                                             (const char*)d_in[6], (const char*)d_in[7], (const char*)d_in[8],
                                             (char*)d_out, (char*)d_ws);
}
```
